```python
import torch

def _softmax(x, axis=-1):
    return torch.softmax(x, dim=axis)
def _gelu(x):
    return torch.nn.functional.gelu(x, approximate="tanh")
def _silu(x):
    return torch.nn.functional.silu(x)
def _sigmoid(x):
    return torch.sigmoid(x)
def _layernorm(x, axis=-1, eps=1e-5):
    m = x.mean(dim=axis, keepdim=True)
    v = x.var(dim=axis, keepdim=True, unbiased=False)
    return (x - m) / torch.sqrt(v + eps)
def _scatter_add(tgt, idx, src, axis=0):
    return tgt.clone().index_add_(axis, idx, src)
def _segment_sum(src, seg, n):
    out = torch.zeros((n,) + src.shape[1:], dtype=src.dtype)
    return out.index_add_(0, seg, src)
def _pad(x, pw):
    flat = tuple(v for pair in reversed(pw) for v in pair)
    return torch.nn.functional.pad(x, flat)

kernel_name = 'interleave_layers__grouped_kv_mqa__sliding_local_dilated__prompt'

def setup_inputs():

    _w = (lambda a: (lambda t: t.to(torch.bfloat16) if t.is_floating_point() else t)(torch.as_tensor(a)))
    def _A_setup():
        B, S, D, H = 1, 2048, 576, 32
        Dh = D // H
        return {
            "x": _w((lambda *s: torch.randn(*s, dtype=torch.bfloat16) * 0.05)(B, S, D)),
            "wq": _w((lambda *s: torch.randn(*s, dtype=torch.bfloat16) * 0.05)(D, H * Dh)),
            "wk": _w((lambda *s: torch.randn(*s, dtype=torch.bfloat16) * 0.05)(D, Dh)),
            "wv": _w((lambda *s: torch.randn(*s, dtype=torch.bfloat16) * 0.05)(D, Dh)),
            "wo": _w((lambda *s: torch.randn(*s, dtype=torch.bfloat16) * 0.05)(H * Dh, D)),
        }
    _A_in = _A_setup()

    def _B_setup():
        B, S, D = 1, 2048, 576
        return {
            "x": _w((lambda *s: torch.randn(*s, dtype=torch.bfloat16) * 0.05)(B, S, D)),
            "wqkv": _w((lambda *s: torch.randn(*s, dtype=torch.bfloat16) * 0.05)(D, 3 * D)),
            "wo": _w((lambda *s: torch.randn(*s, dtype=torch.bfloat16) * 0.05)(D, D)),
        }
    _B_in = _B_setup()

    return {
        "A_x": _A_in["x"],
        "A_wq": _A_in["wq"],
        "A_wk": _A_in["wk"],
        "A_wv": _A_in["wv"],
        "A_wo": _A_in["wo"],
        "B_x": _B_in["x"],
        "B_wqkv": _B_in["wqkv"],
        "B_wo": _B_in["wo"],
    }

def reference(A_x, A_wq, A_wk, A_wv, A_wo, B_x, B_wqkv, B_wo):
    def _A_ref(x, wq, wk, wv, wo):
        B, S, D = x.shape
        H = 32
        Dh = D // H
        q = (lambda x, *d: x.permute(*d))((x @ wq).reshape(B, S, H, Dh), 0, 2, 1, 3)
        k = x @ wk
        v = x @ wv
        scores = torch.einsum("bhqd,bkd->bhqk", q, k) * (Dh ** -0.5)
        p = _softmax(scores, axis=-1)
        o = torch.einsum("bhqk,bkd->bhqd", p, v)
        return (lambda x, *d: x.permute(*d))(o, 0, 2, 1, 3).reshape(B, S, H * Dh) @ wo

    def _B_ref(x, wqkv, wo):
        B, S, D = x.shape
        H = 32; Dh = D // H
        W, dil = 8, 2
        qkv = (lambda x, *d: x.permute(*d))((x @ wqkv).reshape(B, S, 3, H, Dh), 2, 0, 3, 1, 4)
        q, k, v = qkv[0], qkv[1], qkv[2]
        scale = Dh ** -0.5
        pos = torch.arange(S)
        off = torch.arange(W) * dil
        idx = pos[:, None] - off[None, :]
        mask = idx >= 0
        idx_c = torch.where(mask, idx, 0).reshape(-1)
        kg = k[:, :, idx_c, :].reshape(B, H, S, W, Dh)
        vg = v[:, :, idx_c, :].reshape(B, H, S, W, Dh)
        logits = torch.einsum("bhsd,bhswd->bhsw", q, kg) * scale
        logits = torch.where(mask[None, None, :, :], logits, -1e9)
        attn = _softmax(logits, axis=-1)
        o = torch.einsum("bhsw,bhswd->bhsd", attn, vg)
        return (lambda x, *d: x.permute(*d))(o, 0, 2, 1, 3).reshape(B, S, D) @ wo

    x = A_x
    x = x + _A_ref(_layernorm(x, axis=-1), A_wq, A_wk, A_wv, A_wo)
    x = x + _B_ref(_layernorm(x, axis=-1), B_wqkv, B_wo)
    x = x + _A_ref(_layernorm(x, axis=-1), A_wq, A_wk, A_wv, A_wo)
    x = x + _B_ref(_layernorm(x, axis=-1), B_wqkv, B_wo)
    return x
```

```cpp
#include <hip/hip_runtime.h>
#include <hip/hip_bf16.h>
#include <stdint.h>

typedef __attribute__((ext_vector_type(8))) short bf16x8;
typedef __attribute__((ext_vector_type(4))) float f32x4;
typedef __attribute__((ext_vector_type(2))) float f32x2;
typedef __attribute__((ext_vector_type(16))) float f32x16;
typedef __attribute__((ext_vector_type(4))) unsigned u32x4;
typedef __attribute__((ext_vector_type(2))) unsigned u32x2;
typedef __attribute__((ext_vector_type(2))) __bf16 bf16x2_t;
typedef unsigned short u16;

#define DI __device__ __forceinline__

constexpr int SEQ = 2048;
constexpr int DM = 576;
constexpr int FRAG_TILE = 18 * 1024;

constexpr size_t OFF_WA = 0;
constexpr size_t OFF_WOA = OFF_WA + 39 * (size_t)FRAG_TILE;
constexpr size_t OFF_WB = OFF_WOA + 36 * (size_t)FRAG_TILE;
constexpr size_t OFF_WOB = OFF_WB + 112 * (size_t)FRAG_TILE;
constexpr size_t OFF_Q = OFF_WOB + 36 * (size_t)FRAG_TILE;
constexpr size_t OFF_KV = OFF_Q + (size_t)SEQ * 32 * 48;
constexpr size_t OFF_O = OFF_KV + 159744;
constexpr size_t ACT_BYTES = (size_t)SEQ * DM * 2;
constexpr size_t OFF_X1 = OFF_O + ACT_BYTES;
constexpr size_t OFF_X2 = OFF_X1 + ACT_BYTES;
constexpr size_t OFF_X3 = OFF_X2 + ACT_BYTES;

constexpr int KV_K0 = 0;
constexpr int KV_K1 = 65536;
constexpr int KV_V = 65536 + 16384;
constexpr int KV_BYTES = 159744;

DI float bf_lo(unsigned u) { return __uint_as_float(u << 16); }
DI float bf_hi(unsigned u) { return __uint_as_float(u & 0xffff0000u); }
DI unsigned pack_bf16(float a, float b) {
  f32x2 v = {a, b};
  bf16x2_t r = __builtin_convertvector(v, bf16x2_t);
  return __builtin_bit_cast(unsigned, r);
}
DI float round_bf16(float a) { return bf_lo(pack_bf16(a, 0.f)); }

__global__ __launch_bounds__(256) void prep_kernel(const u16* __restrict__ wq, const u16* __restrict__ wk,
                                                   const u16* __restrict__ wv, const u16* __restrict__ woa,
                                                   const u16* __restrict__ wqkv, const u16* __restrict__ wob,
                                                   char* __restrict__ ws) {
  const int wid = blockIdx.x * 4 + (threadIdx.x >> 6);
  const int lane = threadIdx.x & 63;
  if (wid >= 223 * 18) return;
  const int tile = wid / 18, s = wid % 18;
  const int c = lane & 15;
  const u16* src = nullptr;
  int ld = 0;
  char* dst;
  if (tile < 39) {
    dst = ws + OFF_WA + (size_t)tile * FRAG_TILE;
    int n = tile * 16 + c;
    if (n < 576) { src = wq + n; ld = 576; }
    else if (n < 594) { src = wk + (n - 576); ld = 18; }
    else if (n < 612) { src = wv + (n - 594); ld = 18; }
  } else if (tile < 75) {
    int t = tile - 39;
    dst = ws + OFF_WOA + (size_t)t * FRAG_TILE;
    src = woa + t * 16 + c; ld = 576;
  } else if (tile < 187) {
    int t = tile - 75;
    dst = ws + OFF_WB + (size_t)t * FRAG_TILE;
    int g = t / 14, n = (t % 14) * 16 + c;
    if (n < 216) { src = wqkv + (n / 72) * 576 + g * 72 + (n % 72); ld = 1728; }
  } else {
    int t = tile - 187;
    dst = ws + OFF_WOB + (size_t)t * FRAG_TILE;
    src = wob + t * 16 + c; ld = 576;
  }
  const int k0 = 32 * s + 8 * (lane >> 4);
  u32x4 v = {0, 0, 0, 0};
  if (src) {
#pragma unroll
    for (int j = 0; j < 4; ++j) {
      unsigned lo = src[(size_t)(k0 + 2 * j) * ld];
      unsigned hi = src[(size_t)(k0 + 2 * j + 1) * ld];
      v[j] = lo | (hi << 16);
    }
  }
  *(u32x4*)(dst + s * 1024 + lane * 16) = v;
}

constexpr int LDA = 1184;

template <int MODE>
struct GemmCfg {
  static constexpr int ROWS = (MODE == 2) ? 80 : 64;
  static constexpr int RT = ROWS / 16;
  static constexpr int NT = (MODE == 2) ? 14 : 6;
  static constexpr int KS = (MODE == 2) ? 1 : 2;
  static constexpr int NW = NT * KS;
  static constexpr int KSTEPS = 18 / KS;
  static constexpr int CB = (MODE == 0) ? 7 : (MODE == 1 ? 6 : 8);
};

template <int MODE>
__global__ __launch_bounds__(GemmCfg<MODE>::NW * 64) void gemm_kernel(const u16* __restrict__ A, const char* __restrict__ Wf,
                                                                      const u16* __restrict__ resid, u16* __restrict__ out,
                                                                      char* __restrict__ qout, char* __restrict__ kvout) {
  using C = GemmCfg<MODE>;
  __shared__ __attribute__((aligned(16))) char smem[C::ROWS * LDA + (C::KS == 2 ? C::NT * C::RT * 1024 : 0)];
  const int tid = threadIdx.x, lane = tid & 63, wave = tid >> 6;
  const int xcd = blockIdx.x & 7, idx = blockIdx.x >> 3;
  const int rb = xcd * 4 + idx / C::CB, cb = idx % C::CB;
  const int tcol = wave % C::NT, kh = wave / C::NT;
  const int gtile = cb * C::NT + tcol;
  const bool active = (MODE != 0) || (gtile < 39);

  bf16x8 wf[C::KSTEPS];
  auto load_w = [&]() {
    if (active) {
      const char* wp = Wf + (size_t)gtile * FRAG_TILE + (size_t)kh * C::KSTEPS * 1024 + lane * 16;
#pragma unroll
      for (int s = 0; s < C::KSTEPS; ++s) wf[s] = *(const bf16x8*)(wp + s * 1024);
    } else {
#pragma unroll
      for (int s = 0; s < C::KSTEPS; ++s) wf[s] = bf16x8{0, 0, 0, 0, 0, 0, 0, 0};
    }
  };
  if (MODE != 2) load_w();

  const int row0 = rb * 64 - (MODE == 2 ? 16 : 0);
  if (wave < C::ROWS / 8) {
    const int r = wave * 8 + (lane >> 3), c = lane & 7;
    int grow = row0 + r;
    if (grow < 0) grow = 0;
    const u16* ap = A + (size_t)grow * DM + c * 8;
    u32x4 xa[9];
#pragma unroll
    for (int i = 0; i < 9; ++i) xa[i] = *(const u32x4*)(ap + i * 64);
    if (MODE != 1) {
      float sum = 0.f;
#pragma unroll
      for (int i = 0; i < 9; ++i)
#pragma unroll
        for (int j = 0; j < 4; ++j) sum += bf_lo(xa[i][j]) + bf_hi(xa[i][j]);
      sum += __shfl_xor(sum, 1);
      sum += __shfl_xor(sum, 2);
      sum += __shfl_xor(sum, 4);
      const float mean = sum * (1.f / 576.f);
      float var = 0.f;
#pragma unroll
      for (int i = 0; i < 9; ++i)
#pragma unroll
        for (int j = 0; j < 4; ++j) {
          float a = bf_lo(xa[i][j]) - mean, b = bf_hi(xa[i][j]) - mean;
          var += a * a + b * b;
        }
      var += __shfl_xor(var, 1);
      var += __shfl_xor(var, 2);
      var += __shfl_xor(var, 4);
      const float rstd = 1.0f / sqrtf(var * (1.f / 576.f) + 1e-5f);
#pragma unroll
      for (int i = 0; i < 9; ++i)
#pragma unroll
        for (int j = 0; j < 4; ++j)
          xa[i][j] = pack_bf16((bf_lo(xa[i][j]) - mean) * rstd, (bf_hi(xa[i][j]) - mean) * rstd);
    }
    char* lp = smem + r * LDA + c * 16;
#pragma unroll
    for (int i = 0; i < 9; ++i) *(u32x4*)(lp + i * 128) = xa[i];
  }
  if (MODE == 2) {
    __builtin_amdgcn_sched_barrier(0);
    load_w();
  }
  __syncthreads();

  f32x4 acc[C::RT];
#pragma unroll
  for (int t = 0; t < C::RT; ++t) acc[t] = f32x4{0.f, 0.f, 0.f, 0.f};
  {
    const char* ap = smem + (lane & 15) * LDA + (lane >> 4) * 16 + kh * C::KSTEPS * 64;
#pragma unroll
    for (int s = 0; s < C::KSTEPS; ++s) {
#pragma unroll
      for (int t = 0; t < C::RT; ++t) {
        bf16x8 af = *(const bf16x8*)(ap + t * 16 * LDA + s * 64);
        acc[t] = __builtin_amdgcn_mfma_f32_16x16x32_bf16(wf[s], af, acc[t], 0, 0, 0);
      }
    }
  }
  if (C::KS == 2) {
    char* red = smem + C::ROWS * LDA + tcol * C::RT * 1024 + lane * 16;
    if (kh == 1) {
#pragma unroll
      for (int t = 0; t < C::RT; ++t) *(f32x4*)(red + t * 1024) = acc[t];
    }
    __syncthreads();
    if (kh == 1) return;
#pragma unroll
    for (int t = 0; t < C::RT; ++t) acc[t] += *(const f32x4*)(red + t * 1024);
  }

  const int mloc = lane & 15, nq = (lane >> 4) * 4;
  if (MODE == 1) {
    const int n = gtile * 16 + nq;
#pragma unroll
    for (int t = 0; t < C::RT; ++t) {
      const size_t off = (size_t)(row0 + t * 16 + mloc) * DM + n;
      u32x2 xr = *(const u32x2*)(resid + off);
      float r0 = round_bf16(acc[t][0]), r1 = round_bf16(acc[t][1]), r2 = round_bf16(acc[t][2]), r3 = round_bf16(acc[t][3]);
      u32x2 o;
      o[0] = pack_bf16(bf_lo(xr[0]) + r0, bf_hi(xr[0]) + r1);
      o[1] = pack_bf16(bf_lo(xr[1]) + r2, bf_hi(xr[1]) + r3);
      *(u32x2*)(out + off) = o;
    }
  } else if (MODE == 0) {
    if (!active) return;
    const float qs = 0.23570226039551584f * 1.4426950408889634f;
    if (gtile < 36) {
#pragma unroll
      for (int t = 0; t < C::RT; ++t) {
        const int pos = row0 + t * 16 + mloc;
#pragma unroll
        for (int i = 0; i < 4; ++i) {
          const int n = gtile * 16 + nq + i;
          const int h = n / 18, d = n % 18;
          u16* qp = (u16*)(qout + ((size_t)pos * 32 + h) * 48);
          qp[d] = (u16)(pack_bf16(acc[t][i] * qs, 0.f) & 0xffffu);
          if (d == 17) {
            *(unsigned*)(qp + 18) = 0u;
            *(u32x2*)(qp + 20) = u32x2{0u, 0u};
          }
        }
      }
    } else {
#pragma unroll
      for (int t = 0; t < C::RT; ++t) {
        const int pos = row0 + t * 16 + mloc;
        const int kt = pos >> 5, r = pos & 31;
#pragma unroll
        for (int i = 0; i < 4; ++i) {
          const int n = (gtile - 36) * 16 + nq + i;
          const u16 v = (u16)(pack_bf16(acc[t][i], 0.f) & 0xffffu);
          if (n < 16) {
            *(u16*)(kvout + KV_K0 + kt * 1024 + (n >> 3) * 512 + r * 16 + (n & 7) * 2) = v;
          } else if (n < 18) {
            *(u16*)(kvout + KV_K1 + kt * 256 + r * 8 + (n - 16) * 2) = v;
            if (n == 17) *(unsigned*)(kvout + KV_K1 + kt * 256 + r * 8 + 4) = 0x00003f80u;
          } else if (n < 37) {
            const int d = n - 18;
            const int s = r >> 4, rr = r & 15;
            const int hh = (rr >> 2) & 1, j = ((rr >> 3) << 2) | (rr & 3);
            *(u16*)(kvout + KV_V + (kt * 2 + s) * 608 + hh * 304 + d * 16 + j * 2) = (d == 18) ? (u16)0x3f80 : v;
          }
        }
      }
    }
  } else {
    constexpr int LDQ = 456;
    __syncthreads();
#pragma unroll
    for (int t = 0; t < C::RT; ++t) {
      u32x2 o;
      o[0] = pack_bf16(acc[t][0], acc[t][1]);
      o[1] = pack_bf16(acc[t][2], acc[t][3]);
      *(u32x2*)(smem + (t * 16 + mloc) * LDQ + (tcol * 16 + nq) * 2) = o;
    }
    __syncthreads();
    if (tid < 512) {
      const int half = tid & 1, hh = (tid >> 1) & 3, i = tid >> 3;
      const int pos = rb * 64 + i;
      const char* qrow = smem + (16 + i) * LDQ + hh * 36;
      float q[18];
#pragma unroll
      for (int d = 0; d < 9; ++d) {
        unsigned u = *(const unsigned*)(qrow + d * 4);
        q[2 * d] = bf_lo(u);
        q[2 * d + 1] = bf_hi(u);
      }
      const char* kbase = qrow - 8 * half * LDQ;
      float lg[4];
      float mx = -3.0e38f;
#pragma unroll
      for (int w = 0; w < 4; ++w) {
        const char* krow = kbase - 2 * w * LDQ + 144;
        float sacc = 0.f;
#pragma unroll
        for (int d = 0; d < 9; ++d) {
          unsigned u = *(const unsigned*)(krow + d * 4);
          sacc += q[2 * d] * bf_lo(u) + q[2 * d + 1] * bf_hi(u);
        }
        sacc *= 0.23570226039551584f;
        lg[w] = (pos - 2 * (w + 4 * half) >= 0) ? sacc : -1e9f;
        mx = fmaxf(mx, lg[w]);
      }
      mx = fmaxf(mx, __shfl_xor(mx, 1));
      float den = 0.f;
#pragma unroll
      for (int w = 0; w < 4; ++w) {
        lg[w] = __expf(lg[w] - mx);
        den += lg[w];
      }
      den += __shfl_xor(den, 1);
      const float inv = 1.f / den;
      float o[18];
#pragma unroll
      for (int d = 0; d < 18; ++d) o[d] = 0.f;
#pragma unroll
      for (int w = 0; w < 4; ++w) {
        const char* vrow = kbase - 2 * w * LDQ + 288;
        const float p = lg[w] * inv;
#pragma unroll
        for (int d = 0; d < 9; ++d) {
          unsigned u = *(const unsigned*)(vrow + d * 4);
          o[2 * d] += p * bf_lo(u);
          o[2 * d + 1] += p * bf_hi(u);
        }
      }
#pragma unroll
      for (int d = 0; d < 18; ++d) o[d] += __shfl_xor(o[d], 1);
      unsigned* op = (unsigned*)(out + (size_t)pos * DM + (cb * 4 + hh) * 18);
      if (half == 0) {
#pragma unroll
        for (int d = 0; d < 5; ++d) op[d] = pack_bf16(o[2 * d], o[2 * d + 1]);
      } else {
#pragma unroll
        for (int d = 5; d < 9; ++d) op[d] = pack_bf16(o[2 * d], o[2 * d + 1]);
      }
    }
  }
}

__global__ __launch_bounds__(512) void attn_kernel(const char* __restrict__ qin, const char* __restrict__ kvin,
                                                   u16* __restrict__ out) {
  __shared__ __attribute__((aligned(16))) char smem[KV_BYTES];
  const int tid = threadIdx.x, lane = tid & 63, wave = tid >> 6;
  const int r = lane & 31, h = lane >> 5;
  for (int i = wave; i < KV_BYTES / 1024; i += 8) {
    __builtin_amdgcn_global_load_lds((const unsigned*)(kvin + i * 1024 + lane * 16), (unsigned*)(smem + i * 1024), 16, 0, 0);
  }
  const int pos = blockIdx.x * 8 + wave;
  const char* qp = qin + ((size_t)pos * 32 + r) * 48;
  bf16x8 qf0 = *(const bf16x8*)(qp + h * 16);
  u32x4 q1 = *(const u32x4*)(qp + 32);
  if (h) q1 = u32x4{0u, 0u, 0u, 0u};
  asm volatile("s_waitcnt vmcnt(0)" ::: "memory");
  __syncthreads();

  const char* k0p = smem + KV_K0 + h * 512 + r * 16;
  const char* k1p = smem + KV_K1 + r * 8;
  const char* vp = smem + KV_V + h * 304 + (r < 18 ? r : 18) * 16;

  f32x16 zero;
#pragma unroll
  for (int i = 0; i < 16; ++i) zero[i] = 0.f;
  f32x16 acc = zero;
  float mref;
  {
    bf16x8 k0 = *(const bf16x8*)(k0p);
    u32x2 k1l = *(const u32x2*)(k1p);
    u32x4 k1 = {k1l[0], k1l[1], 0u, 0u};
    f32x16 s = __builtin_amdgcn_mfma_f32_32x32x16_bf16(k0, qf0, zero, 0, 0, 0);
    s = __builtin_amdgcn_mfma_f32_32x32x16_bf16(__builtin_bit_cast(bf16x8, k1), __builtin_bit_cast(bf16x8, q1), s, 0, 0, 0);
    float mx = s[0];
#pragma unroll
    for (int i = 1; i < 16; ++i) mx = fmaxf(mx, s[i]);
    mx = fmaxf(mx, __shfl_xor(mx, 32));
    mref = round_bf16(mx + 7.0f);
    if (!h) q1[1] = (q1[1] & 0xffff0000u) | (pack_bf16(-mref, 0.f) & 0xffffu);
  }

  for (int t = 0; t < 64; ++t) {
    bf16x8 k0 = *(const bf16x8*)(k0p + t * 1024);
    u32x2 k1l = *(const u32x2*)(k1p + t * 256);
    u32x4 k1 = {k1l[0], k1l[1], 0u, 0u};
    bf16x8 v0 = *(const bf16x8*)(vp + (2 * t) * 608);
    bf16x8 v1 = *(const bf16x8*)(vp + (2 * t + 1) * 608);
    f32x16 s = __builtin_amdgcn_mfma_f32_32x32x16_bf16(k0, qf0, zero, 0, 0, 0);
    s = __builtin_amdgcn_mfma_f32_32x32x16_bf16(__builtin_bit_cast(bf16x8, k1), __builtin_bit_cast(bf16x8, q1), s, 0, 0, 0);
    u32x4 p0, p1;
#pragma unroll
    for (int i = 0; i < 4; ++i) {
      p0[i] = pack_bf16(__builtin_amdgcn_exp2f(s[2 * i]), __builtin_amdgcn_exp2f(s[2 * i + 1]));
      p1[i] = pack_bf16(__builtin_amdgcn_exp2f(s[8 + 2 * i]), __builtin_amdgcn_exp2f(s[8 + 2 * i + 1]));
    }
    unsigned orv = (p0[0] | p0[1]) | (p0[2] | p0[3]) | (p1[0] | p1[1]) | (p1[2] | p1[3]);
    if (__any((orv & 0x40004000u) != 0u)) {
      float mx = s[0];
#pragma unroll
      for (int i = 1; i < 16; ++i) mx = fmaxf(mx, s[i]);
      mx = fmaxf(mx, __shfl_xor(mx, 32));
      const float mnew = fmaxf(mref, round_bf16(mx + mref + 7.0f));
      const float delta = mnew - mref;
      const float sc = __builtin_amdgcn_exp2f(-delta);
#pragma unroll
      for (int i = 0; i < 16; ++i) acc[i] *= sc;
      mref = mnew;
      if (!h) q1[1] = (q1[1] & 0xffff0000u) | (pack_bf16(-mref, 0.f) & 0xffffu);
#pragma unroll
      for (int i = 0; i < 4; ++i) {
        p0[i] = pack_bf16(__builtin_amdgcn_exp2f(s[2 * i] - delta), __builtin_amdgcn_exp2f(s[2 * i + 1] - delta));
        p1[i] = pack_bf16(__builtin_amdgcn_exp2f(s[8 + 2 * i] - delta), __builtin_amdgcn_exp2f(s[8 + 2 * i + 1] - delta));
      }
    }
    acc = __builtin_amdgcn_mfma_f32_32x32x16_bf16(v0, __builtin_bit_cast(bf16x8, p0), acc, 0, 0, 0);
    acc = __builtin_amdgcn_mfma_f32_32x32x16_bf16(v1, __builtin_bit_cast(bf16x8, p1), acc, 0, 0, 0);
  }
  float l = acc[10];
  float lo = __shfl_xor(l, 32);
  if (h) l = lo;
  const float inv = 1.f / l;
  u16* op = out + (size_t)pos * DM + r * 18;
  {
    unsigned a0 = pack_bf16(acc[0] * inv, acc[1] * inv), a1 = pack_bf16(acc[2] * inv, acc[3] * inv);
    unsigned b0 = pack_bf16(acc[4] * inv, acc[5] * inv), b1 = pack_bf16(acc[6] * inv, acc[7] * inv);
    unsigned* o32 = (unsigned*)(op + h * 4);
    o32[0] = a0;
    o32[1] = a1;
    o32[4] = b0;
    o32[5] = b1;
    if (!h) *(unsigned*)(op + 16) = pack_bf16(acc[8] * inv, acc[9] * inv);
  }
}

extern "C" void kernel_launch(void* const* d_in, const int* in_sizes, int n_in, void* d_out, int out_size, void* d_ws,
                              size_t ws_size, hipStream_t stream) {
  const u16* x0 = (const u16*)d_in[0];
  const u16* wq = (const u16*)d_in[1];
  const u16* wk = (const u16*)d_in[2];
  const u16* wv = (const u16*)d_in[3];
  const u16* woa = (const u16*)d_in[4];
  const u16* wqkv = (const u16*)d_in[6];
  const u16* wob = (const u16*)d_in[7];
  char* ws = (char*)d_ws;
  u16* obuf = (u16*)(ws + OFF_O);
  u16* x1 = (u16*)(ws + OFF_X1);
  u16* x2 = (u16*)(ws + OFF_X2);
  u16* x3 = (u16*)(ws + OFF_X3);
  char* qb = ws + OFF_Q;
  char* kvb = ws + OFF_KV;

  prep_kernel<<<(223 * 18 + 3) / 4, 256, 0, stream>>>(wq, wk, wv, woa, wqkv, wob, ws);
  for (int layer = 0; layer < 2; ++layer) {
    const u16* xin = layer == 0 ? x0 : x2;
    u16* xmid = layer == 0 ? x1 : x3;
    u16* xout = layer == 0 ? x2 : (u16*)d_out;
    gemm_kernel<0><<<224, 768, 0, stream>>>(xin, ws + OFF_WA, nullptr, nullptr, qb, kvb);
    attn_kernel<<<256, 512, 0, stream>>>(qb, kvb, obuf);
    gemm_kernel<1><<<192, 768, 0, stream>>>(obuf, ws + OFF_WOA, xin, xmid, nullptr, nullptr);
    gemm_kernel<2><<<256, 896, 0, stream>>>(xmid, ws + OFF_WB, nullptr, obuf, nullptr, nullptr);
    gemm_kernel<1><<<192, 768, 0, stream>>>(obuf, ws + OFF_WOB, xmid, xout, nullptr, nullptr);
  }
}
```

```cpp
#include <hip/hip_runtime.h>
#include <hip/hip_bf16.h>
#include <stdint.h>

typedef __attribute__((ext_vector_type(8))) short bf16x8;
typedef __attribute__((ext_vector_type(4))) float f32x4;
typedef __attribute__((ext_vector_type(2))) float f32x2;
typedef __attribute__((ext_vector_type(16))) float f32x16;
typedef __attribute__((ext_vector_type(4))) unsigned u32x4;
typedef __attribute__((ext_vector_type(4), aligned(4))) unsigned u32x4_a4;
typedef __attribute__((ext_vector_type(2))) unsigned u32x2;
typedef __attribute__((ext_vector_type(2))) __bf16 bf16x2_t;
typedef unsigned short u16;

#define DI __device__ __forceinline__

constexpr int SEQ = 2048;
constexpr int DM = 576;
constexpr int FRAG_TILE = 18 * 1024;

constexpr int REC = 2368;
constexpr int REC_K1 = 1024;
constexpr int REC_V = 1152;
constexpr int KV_BYTES = 64 * REC;
constexpr int CHUNK = 16 * REC;

constexpr size_t OFF_WA = 0;
constexpr size_t OFF_WOA = OFF_WA + 39 * (size_t)FRAG_TILE;
constexpr size_t OFF_WB = OFF_WOA + 36 * (size_t)FRAG_TILE;
constexpr size_t OFF_WOB = OFF_WB + 112 * (size_t)FRAG_TILE;
constexpr size_t ACT_BYTES = (size_t)SEQ * DM * 2;
constexpr size_t OFF_Q = OFF_WOB + 36 * (size_t)FRAG_TILE;
constexpr size_t OFF_KV = OFF_Q + ACT_BYTES;
constexpr size_t OFF_O = OFF_KV + KV_BYTES;
constexpr size_t OFF_X1 = OFF_O + ACT_BYTES;
constexpr size_t OFF_X2 = OFF_X1 + ACT_BYTES;
constexpr size_t OFF_X3 = OFF_X2 + ACT_BYTES;

DI float bf_lo(unsigned u) { return __uint_as_float(u << 16); }
DI float bf_hi(unsigned u) { return __uint_as_float(u & 0xffff0000u); }
DI unsigned pack_bf16(float a, float b) {
  f32x2 v = {a, b};
  bf16x2_t r = __builtin_convertvector(v, bf16x2_t);
  return __builtin_bit_cast(unsigned, r);
}
DI float round_bf16(float a) { return bf_lo(pack_bf16(a, 0.f)); }
DI float dot2_bf16(unsigned a, unsigned b, float c) {
  return __builtin_amdgcn_fdot2_f32_bf16(__builtin_bit_cast(bf16x2_t, a), __builtin_bit_cast(bf16x2_t, b), c, false);
}

constexpr int PREP_FAST = 55 * 18;
constexpr int PREP_WAVES = PREP_FAST + 3 * 18;
__global__ __launch_bounds__(256) void prep_kernel(const u16* __restrict__ wq, const u16* __restrict__ wk,
                                                   const u16* __restrict__ wv, const u16* __restrict__ woa,
                                                   const u16* __restrict__ wqkv, const u16* __restrict__ wob,
                                                   char* __restrict__ ws) {
  const int wid = blockIdx.x * 4 + (threadIdx.x >> 6);
  const int lane = threadIdx.x & 63;
  if (wid >= PREP_WAVES) return;
  const int g = lane >> 4, c = lane & 15;
  if (wid < PREP_FAST) {
    const int grp = wid / 18, s = wid % 18;
    const u16* src = nullptr;
    int ld = 576;
    char* dst;
    if (grp < 9) {
      dst = ws + OFF_WA + (size_t)grp * 4 * FRAG_TILE;
      src = wq + grp * 64 + c * 4;
    } else if (grp < 18) {
      dst = ws + OFF_WOA + (size_t)(grp - 9) * 4 * FRAG_TILE;
      src = woa + (grp - 9) * 64 + c * 4;
    } else if (grp < 27) {
      dst = ws + OFF_WOB + (size_t)(grp - 18) * 4 * FRAG_TILE;
      src = wob + (grp - 18) * 64 + c * 4;
    } else {
      dst = ws + OFF_WB + (size_t)(grp - 27) * 4 * FRAG_TILE;
      const int np = (grp - 27) * 64 + c * 4;
      const int hg = np / 224, n = np % 224;
      ld = 1728;
      if (n < 216) src = wqkv + (n / 72) * 576 + hg * 72 + (n % 72);
    }
    u32x2 r[8];
    const int k0 = 32 * s + 8 * g;
#pragma unroll
    for (int j = 0; j < 8; ++j) r[j] = src ? *(const u32x2*)(src + (size_t)(k0 + j) * ld) : u32x2{0u, 0u};
    char* dp = dst + (size_t)(c >> 2) * FRAG_TILE + s * 1024 + (g * 16 + (c & 3) * 4) * 16;
#pragma unroll
    for (int q = 0; q < 4; ++q) {
      u32x4 v;
#pragma unroll
      for (int j = 0; j < 4; ++j)
        v[j] = __builtin_amdgcn_perm(r[2 * j + 1][q >> 1], r[2 * j][q >> 1], (q & 1) ? 0x07060302u : 0x05040100u);
      *(u32x4*)(dp + q * 16) = v;
    }
  } else {
    const int w2 = wid - PREP_FAST;
    const int tile = 36 + w2 / 18, s = w2 % 18;
    const int n = tile * 16 + c;
    const u16* src = nullptr;
    if (n < 594) src = wk + (n - 576);
    else if (n < 612) src = wv + (n - 594);
    const int k0 = 32 * s + 8 * g;
    u32x4 v = {0, 0, 0, 0};
    if (src) {
#pragma unroll
      for (int j = 0; j < 4; ++j) {
        unsigned lo = src[(size_t)(k0 + 2 * j) * 18];
        unsigned hi = src[(size_t)(k0 + 2 * j + 1) * 18];
        v[j] = lo | (hi << 16);
      }
    }
    *(u32x4*)(ws + OFF_WA + (size_t)tile * FRAG_TILE + s * 1024 + lane * 16) = v;
  }
}

template <int MODE>
struct GemmCfg {
  static constexpr int ROWS = (MODE == 2) ? 80 : 64;
  static constexpr int RT = ROWS / 16;
  static constexpr int NT = (MODE == 2) ? 14 : 6;
  static constexpr int KS = (MODE == 2) ? 1 : 2;
  static constexpr int NW = NT * KS;
  static constexpr int KSTEPS = 18 / KS;
  static constexpr int CB = (MODE == 0) ? 7 : (MODE == 1 ? 6 : 8);
  static constexpr int NACC = RT + (MODE == 1 ? 0 : 1);
  static constexpr int OFF_STAT = ROWS * 1152;
  static constexpr int OFF_RED = OFF_STAT + ROWS * 8;
  static constexpr int SMEM = OFF_RED + (KS == 2 ? NT * NACC * 1024 : 0);
};

template <int MODE>
__global__ __launch_bounds__(GemmCfg<MODE>::NW * 64) void gemm_kernel(const u16* __restrict__ A, const char* __restrict__ Wf,
                                                                      const u16* __restrict__ resid, u16* __restrict__ out,
                                                                      char* __restrict__ kvout) {
  using C = GemmCfg<MODE>;
  __shared__ __attribute__((aligned(16))) char smem[C::SMEM];
  const int tid = threadIdx.x, lane = tid & 63, wave = tid >> 6;
  const int xcd = blockIdx.x & 7, idx = blockIdx.x >> 3;
  const int rb = xcd * 4 + idx / C::CB, cb = idx % C::CB;
  const int tcol = wave % C::NT, kh = wave / C::NT;
  const int gtile = cb * C::NT + tcol;
  const bool active = (MODE != 0) || (gtile < 39);
  const int row0 = rb * 64 - (MODE == 2 ? 16 : 0);
  const int mloc = lane & 15, g = lane >> 4, nq = g * 4;

  constexpr int NI = C::ROWS * 72 / 64;
#pragma unroll
  for (int ii = 0; ii < (NI + C::NW - 1) / C::NW; ++ii) {
    const int i = wave + ii * C::NW;
    if (i < NI) {
      const int p = i * 64 + lane;
      const int row = p / 72, cc = p - row * 72;
      const int c = cc ^ ((row >> 1) & 7);
      int grow = row0 + row;
      if (MODE == 2) grow = grow < 0 ? 0 : grow;
      __builtin_amdgcn_global_load_lds((const unsigned*)(A + (size_t)grow * DM + c * 8), (unsigned*)(smem + i * 1024), 16, 0, 0);
    }
  }
  bf16x8 wf[C::KSTEPS];
  {
    const char* wp = Wf + (size_t)(active ? gtile : 0) * FRAG_TILE + (size_t)kh * C::KSTEPS * 1024 + lane * 16;
#pragma unroll
    for (int s = 0; s < C::KSTEPS; ++s) wf[s] = *(const bf16x8*)(wp + s * 1024);
  }
  u32x2 xr[C::RT];
  if (MODE == 1) {
#pragma unroll
    for (int t = 0; t < C::RT; ++t) xr[t] = *(const u32x2*)(resid + (size_t)(row0 + t * 16 + mloc) * DM + gtile * 16 + nq);
    asm volatile("s_waitcnt vmcnt(%0)" ::"n"(C::KSTEPS + C::RT) : "memory");
  } else {
    asm volatile("s_waitcnt vmcnt(%0)" ::"n"(C::KSTEPS) : "memory");
  }
  __builtin_amdgcn_s_barrier();
  asm volatile("" ::: "memory");

  if (MODE != 1 && wave < C::ROWS / 8) {
    const int r = wave * 8 + (lane >> 3);
    const char* xp = smem + r * 1152 + (lane & 7) * 16;
    float sum = 0.f;
#pragma unroll
    for (int i = 0; i < 9; ++i) {
      const u32x4 xa = *(const u32x4*)(xp + i * 128);
#pragma unroll
      for (int j = 0; j < 4; ++j) sum += bf_lo(xa[j]) + bf_hi(xa[j]);
      if (i % 3 == 2) __builtin_amdgcn_sched_barrier(0);
    }
    sum += __shfl_xor(sum, 1);
    sum += __shfl_xor(sum, 2);
    sum += __shfl_xor(sum, 4);
    const float mean = sum * (1.f / 576.f);
    asm volatile("" ::: "memory");
    float var = 0.f;
#pragma unroll
    for (int i = 0; i < 9; ++i) {
      const u32x4 xa = *(const u32x4*)(xp + i * 128);
#pragma unroll
      for (int j = 0; j < 4; ++j) {
        float a = bf_lo(xa[j]) - mean, b = bf_hi(xa[j]) - mean;
        var += a * a + b * b;
      }
      if (i % 3 == 2) __builtin_amdgcn_sched_barrier(0);
    }
    var += __shfl_xor(var, 1);
    var += __shfl_xor(var, 2);
    var += __shfl_xor(var, 4);
    const float rstd = 1.0f / sqrtf(var * (1.f / 576.f) + 1e-5f);
    if ((lane & 7) == 0) *(f32x2*)(smem + C::OFF_STAT + r * 8) = f32x2{mean, rstd};
  }

  f32x4 acc[C::NACC];
#pragma unroll
  for (int t = 0; t < C::NACC; ++t) acc[t] = f32x4{0.f, 0.f, 0.f, 0.f};
  {
    const int x = mloc >> 1;
    const int be = (g ^ x) * 16, bo = be ^ 64;
    const char* pA = smem + mloc * 1152 + (kh ? bo + 512 : be);
    const char* pB = smem + mloc * 1152 + (kh ? be + 640 : bo);
    const bf16x8 ones = {0x3f80, 0x3f80, 0x3f80, 0x3f80, 0x3f80, 0x3f80, 0x3f80, 0x3f80};
#pragma unroll
    for (int s = 0; s < C::KSTEPS; ++s) {
      const char* ap = ((s & 1) ? pB : pA) + (s >> 1) * 128;
#pragma unroll
      for (int t = 0; t < C::RT; ++t) {
        bf16x8 af = *(const bf16x8*)(ap + t * 16 * 1152);
        acc[t] = __builtin_amdgcn_mfma_f32_16x16x32_bf16(wf[s], af, acc[t], 0, 0, 0);
      }
      if (MODE != 1) acc[C::RT] = __builtin_amdgcn_mfma_f32_16x16x32_bf16(wf[s], ones, acc[C::RT], 0, 0, 0);
    }
  }
  if (C::KS == 2) {
    char* red = smem + C::OFF_RED + tcol * C::NACC * 1024 + lane * 16;
    if (kh == 1) {
#pragma unroll
      for (int t = 0; t < C::NACC; ++t) *(f32x4*)(red + t * 1024) = acc[t];
    }
    __syncthreads();
    if (kh == 1) return;
#pragma unroll
    for (int t = 0; t < C::NACC; ++t) acc[t] += *(const f32x4*)(red + t * 1024);
  } else {
    __syncthreads();
  }
  if (MODE != 1) {
#pragma unroll
    for (int t = 0; t < C::RT; ++t) {
      const f32x2 st = *(const f32x2*)(smem + C::OFF_STAT + (t * 16 + mloc) * 8);
#pragma unroll
      for (int i = 0; i < 4; ++i) acc[t][i] = (acc[t][i] - st[0] * acc[C::RT][i]) * st[1];
    }
  }

  if (MODE == 1) {
    const int n = gtile * 16 + nq;
#pragma unroll
    for (int t = 0; t < C::RT; ++t) {
      const size_t off = (size_t)(row0 + t * 16 + mloc) * DM + n;
      float r0 = round_bf16(acc[t][0]), r1 = round_bf16(acc[t][1]), r2 = round_bf16(acc[t][2]), r3 = round_bf16(acc[t][3]);
      u32x2 o;
      o[0] = pack_bf16(bf_lo(xr[t][0]) + r0, bf_hi(xr[t][0]) + r1);
      o[1] = pack_bf16(bf_lo(xr[t][1]) + r2, bf_hi(xr[t][1]) + r3);
      *(u32x2*)(out + off) = o;
    }
  } else if (MODE == 0) {
    if (!active) return;
    const float qs = 0.23570226039551584f * 1.4426950408889634f;
    if (gtile < 36) {
#pragma unroll
      for (int t = 0; t < C::RT; ++t) {
        const int pos = row0 + t * 16 + mloc;
        u32x2 o;
        o[0] = pack_bf16(acc[t][0] * qs, acc[t][1] * qs);
        o[1] = pack_bf16(acc[t][2] * qs, acc[t][3] * qs);
        *(u32x2*)(out + (size_t)pos * DM + gtile * 16 + nq) = o;
      }
    } else {
#pragma unroll
      for (int t = 0; t < C::RT; ++t) {
        const int pos = row0 + t * 16 + mloc;
        const int r = pos & 31;
        char* rec = kvout + (pos >> 5) * REC;
        const unsigned w0 = pack_bf16(acc[t][0], acc[t][1]), w1 = pack_bf16(acc[t][2], acc[t][3]);
        if (gtile == 36) {
          *(u32x2*)(rec + (g >> 1) * 512 + r * 16 + (g & 1) * 8) = u32x2{w0, w1};
        } else {
          const int s = r >> 4, rr = r & 15;
          const int hh = (rr >> 2) & 1, j = ((rr >> 3) << 2) | (rr & 3);
          char* vb = rec + REC_V + s * 608 + hh * 304 + j * 2;
          const int d0 = (gtile - 37) * 16 + nq - 2;
          if (d0 < 0) *(unsigned*)(rec + REC_K1 + r * 4) = w0;
          else if (d0 < 18) {
            *(u16*)(vb + d0 * 16) = (u16)(w0 & 0xffffu);
            *(u16*)(vb + (d0 + 1) * 16) = (u16)(w0 >> 16);
          } else if (d0 == 18) *(u16*)(vb + 18 * 16) = (u16)0x3f80;
          if (d0 + 2 < 18) {
            *(u16*)(vb + (d0 + 2) * 16) = (u16)(w1 & 0xffffu);
            *(u16*)(vb + (d0 + 3) * 16) = (u16)(w1 >> 16);
          }
        }
      }
    }
  } else {
    constexpr int LDQ = 456;
#pragma unroll
    for (int t = 0; t < C::RT; ++t) {
      u32x2 o;
      o[0] = pack_bf16(acc[t][0], acc[t][1]);
      o[1] = pack_bf16(acc[t][2], acc[t][3]);
      *(u32x2*)(smem + (t * 16 + mloc) * LDQ + (tcol * 16 + nq) * 2) = o;
    }
    __syncthreads();
    if (tid < 512) {
      const int half = tid & 1, hh = (tid >> 1) & 3, i = tid >> 3;
      const int pos = rb * 64 + i;
      const char* qrow = smem + (16 + i) * LDQ + hh * 36;
      unsigned q[9];
#pragma unroll
      for (int d = 0; d < 9; ++d) q[d] = *(const unsigned*)(qrow + d * 4);
      const char* kbase = qrow - 8 * half * LDQ;
      float lg[4];
      float mx = -3.0e38f;
#pragma unroll
      for (int w = 0; w < 4; ++w) {
        const char* krow = kbase - 2 * w * LDQ + 144;
        float sacc = 0.f;
#pragma unroll
        for (int d = 0; d < 9; ++d) sacc = dot2_bf16(q[d], *(const unsigned*)(krow + d * 4), sacc);
        sacc *= 0.23570226039551584f;
        lg[w] = (pos - 2 * (w + 4 * half) >= 0) ? sacc : -1e9f;
        mx = fmaxf(mx, lg[w]);
      }
      mx = fmaxf(mx, __shfl_xor(mx, 1));
      float den = 0.f;
#pragma unroll
      for (int w = 0; w < 4; ++w) {
        lg[w] = __expf(lg[w] - mx);
        den += lg[w];
      }
      den += __shfl_xor(den, 1);
      const float inv = 1.f / den;
      float o[18];
#pragma unroll
      for (int d = 0; d < 18; ++d) o[d] = 0.f;
#pragma unroll
      for (int w = 0; w < 4; ++w) {
        const char* vrow = kbase - 2 * w * LDQ + 288;
        const float p = lg[w] * inv;
#pragma unroll
        for (int d = 0; d < 9; ++d) {
          unsigned u = *(const unsigned*)(vrow + d * 4);
          o[2 * d] += p * bf_lo(u);
          o[2 * d + 1] += p * bf_hi(u);
        }
      }
#pragma unroll
      for (int d = 0; d < 18; ++d) o[d] += __shfl_xor(o[d], 1);
      unsigned* op = (unsigned*)(out + (size_t)pos * DM + (cb * 4 + hh) * 18);
      if (half == 0) {
#pragma unroll
        for (int d = 0; d < 5; ++d) op[d] = pack_bf16(o[2 * d], o[2 * d + 1]);
      } else {
#pragma unroll
        for (int d = 5; d < 9; ++d) op[d] = pack_bf16(o[2 * d], o[2 * d + 1]);
      }
    }
  }
}

#define MFMA32(a, b, c) __builtin_amdgcn_mfma_f32_32x32x16_bf16(a, b, c, 0, 0, 0)
#define EXP2(x) __builtin_amdgcn_exp2f(x)

__global__ __launch_bounds__(512) void attn_kernel(const u16* __restrict__ qin, const char* __restrict__ kvin,
                                                   u16* __restrict__ out) {
  __shared__ __attribute__((aligned(16))) char smem[66 * REC];
  const int tid = threadIdx.x, lane = tid & 63, wave = tid >> 6;
  const int r = lane & 31, h = lane >> 5;
  u32x4 st[5];
  int soff[5];
#pragma unroll
  for (int j = 0; j < 5; ++j) {
    int i = wave + 8 * j;
    soff[j] = (i > 36 ? 36 : i) * 1024 + lane * 16;
  }
#pragma unroll
  for (int j = 0; j < 5; ++j) st[j] = *(const u32x4*)(kvin + soff[j]);
  const int pos = blockIdx.x * 8 + wave;
  const u16* qp = qin + (size_t)pos * DM + r * 18;
  u32x4 q0u = *(const u32x4_a4*)(qp + h * 8);
  unsigned q1w = *(const unsigned*)(qp + 16);
  if (h) q1w = 0u;
  const bf16x8 q0 = __builtin_bit_cast(bf16x8, q0u);
  const bf16x8 q1 = __builtin_bit_cast(bf16x8, u32x4{q1w, 0u, 0u, 0u});
#pragma unroll
  for (int j = 0; j < 5; ++j) *(u32x4*)(smem + soff[j]) = st[j];
#pragma unroll
  for (int j = 0; j < 5; ++j) st[j] = *(const u32x4*)(kvin + CHUNK + soff[j]);
  __syncthreads();

  const char* k0p = smem + h * 512 + r * 16;
  const char* k1p = smem + REC_K1 + r * 4;
  const char* vp = smem + REC_V + h * 304 + (r < 18 ? r : 18) * 16;

  f32x16 zero;
#pragma unroll
  for (int i = 0; i < 16; ++i) zero[i] = 0.f;
  f32x16 acc = zero;
  f32x16 negm, sA, sB;
  {
    bf16x8 k0 = *(const bf16x8*)(k0p);
    bf16x8 k1 = __builtin_bit_cast(bf16x8, u32x4{*(const unsigned*)(k1p), 0u, 0u, 0u});
    f32x16 s = MFMA32(k0, q0, zero);
    s = MFMA32(k1, q1, s);
    float mx = s[0];
#pragma unroll
    for (int i = 1; i < 16; ++i) mx = fmaxf(mx, s[i]);
    mx = fmaxf(mx, __shfl_xor(mx, 32));
#pragma unroll
    for (int i = 0; i < 16; ++i) negm[i] = -mx;
#pragma unroll
    for (int i = 0; i < 16; ++i) sA[i] = s[i] - mx;
  }
  bf16x8 k0A = *(const bf16x8*)(k0p + REC), k0B;
  unsigned k1A = *(const unsigned*)(k1p + REC), k1B;
  bf16x8 v0A = {0, 0, 0, 0, 0, 0, 0, 0}, v1A = v0A, v0B, v1B;
  u32x4 pA0 = {0u, 0u, 0u, 0u}, pA1 = pA0, pB0, pB1;

#define SB __builtin_amdgcn_sched_barrier(0);
#define TILE_BODY(OFF, SC, SN, K0C, K1C, V0C, V1C, K0N, K1N, V0N, V1N, PP0, PP1, PC0, PC1)                     \
  {                                                                                                            \
    acc = MFMA32(V0C, __builtin_bit_cast(bf16x8, PP0), acc);                                                   \
    K0N = *(const bf16x8*)(k0p + (OFF) + 2 * REC);                                                             \
    K1N = *(const unsigned*)(k1p + (OFF) + 2 * REC);                                                           \
    SB                                                                                                         \
    PC0[0] = pack_bf16(EXP2(SC[0]), EXP2(SC[1]));                                                              \
    PC0[1] = pack_bf16(EXP2(SC[2]), EXP2(SC[3]));                                                              \
    SB                                                                                                         \
    acc = MFMA32(V1C, __builtin_bit_cast(bf16x8, PP1), acc);                                                   \
    V0N = *(const bf16x8*)(vp + (OFF));                                                                        \
    V1N = *(const bf16x8*)(vp + (OFF) + 608);                                                                  \
    SB                                                                                                         \
    PC0[2] = pack_bf16(EXP2(SC[4]), EXP2(SC[5]));                                                              \
    PC0[3] = pack_bf16(EXP2(SC[6]), EXP2(SC[7]));                                                              \
    SB                                                                                                         \
    SN = MFMA32(K0C, q0, negm);                                                                                \
    SB                                                                                                         \
    PC1[0] = pack_bf16(EXP2(SC[8]), EXP2(SC[9]));                                                              \
    PC1[1] = pack_bf16(EXP2(SC[10]), EXP2(SC[11]));                                                            \
    SB                                                                                                         \
    SN = MFMA32(__builtin_bit_cast(bf16x8, u32x4{K1C, 0u, 0u, 0u}), q1, SN);                                   \
    SB                                                                                                         \
    PC1[2] = pack_bf16(EXP2(SC[12]), EXP2(SC[13]));                                                            \
    PC1[3] = pack_bf16(EXP2(SC[14]), EXP2(SC[15]));                                                            \
    SB                                                                                                         \
  }

  for (int t = 0; t < 64; t += 2) {
    if ((t & 15) == 8 && t < 48) {
      const int c = (t >> 4) + 1;
#pragma unroll
      for (int j = 0; j < 5; ++j) *(u32x4*)(smem + c * CHUNK + soff[j]) = st[j];
      if (c < 3) {
#pragma unroll
        for (int j = 0; j < 5; ++j) st[j] = *(const u32x4*)(kvin + (c + 1) * CHUNK + soff[j]);
      }
      asm volatile("s_waitcnt lgkmcnt(0)" ::: "memory");
      __builtin_amdgcn_s_barrier();
      asm volatile("" ::: "memory");
    }
    const int off = t * REC;
    TILE_BODY(off, sA, sB, k0A, k1A, v0A, v1A, k0B, k1B, v0B, v1B, pA0, pA1, pB0, pB1)
    TILE_BODY(off + REC, sB, sA, k0B, k1B, v0B, v1B, k0A, k1A, v0A, v1A, pB0, pB1, pA0, pA1)
  }
  acc = MFMA32(v0A, __builtin_bit_cast(bf16x8, pA0), acc);
  acc = MFMA32(v1A, __builtin_bit_cast(bf16x8, pA1), acc);

  {
    bool bad = !(acc[10] < 1.0e30f);
#pragma unroll
    for (int i = 0; i < 10; ++i) bad = bad || !(fabsf(acc[i]) < 3.0e38f);
    if (__any(bad)) {
      acc = zero;
      float m = -3.0e38f;
      for (int t = 0; t < 64; ++t) {
        bf16x8 k0 = *(const bf16x8*)(k0p + t * REC);
        bf16x8 k1 = __builtin_bit_cast(bf16x8, u32x4{*(const unsigned*)(k1p + t * REC), 0u, 0u, 0u});
        bf16x8 v0 = *(const bf16x8*)(vp + t * REC);
        bf16x8 v1 = *(const bf16x8*)(vp + t * REC + 608);
        f32x16 s = MFMA32(k0, q0, zero);
        s = MFMA32(k1, q1, s);
        float mx = s[0];
#pragma unroll
        for (int i = 1; i < 16; ++i) mx = fmaxf(mx, s[i]);
        mx = fmaxf(mx, __shfl_xor(mx, 32));
        const float mnew = fmaxf(m, mx);
        const float sc = EXP2(m - mnew);
        m = mnew;
#pragma unroll
        for (int i = 0; i < 16; ++i) acc[i] *= sc;
        u32x4 p0, p1;
#pragma unroll
        for (int i = 0; i < 4; ++i) {
          p0[i] = pack_bf16(EXP2(s[2 * i] - m), EXP2(s[2 * i + 1] - m));
          p1[i] = pack_bf16(EXP2(s[8 + 2 * i] - m), EXP2(s[8 + 2 * i + 1] - m));
        }
        acc = MFMA32(v0, __builtin_bit_cast(bf16x8, p0), acc);
        acc = MFMA32(v1, __builtin_bit_cast(bf16x8, p1), acc);
      }
    }
  }
  const float inv = 1.f / acc[10];
  u16* op = out + (size_t)pos * DM + r * 18;
  {
    unsigned a0 = pack_bf16(acc[0] * inv, acc[1] * inv), a1 = pack_bf16(acc[2] * inv, acc[3] * inv);
    unsigned b0 = pack_bf16(acc[4] * inv, acc[5] * inv), b1 = pack_bf16(acc[6] * inv, acc[7] * inv);
    unsigned* o32 = (unsigned*)(op + h * 4);
    o32[0] = a0;
    o32[1] = a1;
    o32[4] = b0;
    o32[5] = b1;
    if (!h) *(unsigned*)(op + 16) = pack_bf16(acc[8] * inv, acc[9] * inv);
  }
}

extern "C" void kernel_launch(void* const* d_in, const int* in_sizes, int n_in, void* d_out, int out_size, void* d_ws,
                              size_t ws_size, hipStream_t stream) {
  const u16* x0 = (const u16*)d_in[0];
  const u16* wq = (const u16*)d_in[1];
  const u16* wk = (const u16*)d_in[2];
  const u16* wv = (const u16*)d_in[3];
  const u16* woa = (const u16*)d_in[4];
  const u16* wqkv = (const u16*)d_in[6];
  const u16* wob = (const u16*)d_in[7];
  char* ws = (char*)d_ws;
  u16* obuf = (u16*)(ws + OFF_O);
  u16* x1 = (u16*)(ws + OFF_X1);
  u16* x2 = (u16*)(ws + OFF_X2);
  u16* x3 = (u16*)(ws + OFF_X3);
  u16* qb = (u16*)(ws + OFF_Q);
  char* kvb = ws + OFF_KV;

  prep_kernel<<<(PREP_WAVES + 3) / 4, 256, 0, stream>>>(wq, wk, wv, woa, wqkv, wob, ws);
  for (int layer = 0; layer < 2; ++layer) {
    const u16* xin = layer == 0 ? x0 : x2;
    u16* xmid = layer == 0 ? x1 : x3;
    u16* xout = layer == 0 ? x2 : (u16*)d_out;
    gemm_kernel<0><<<224, 768, 0, stream>>>(xin, ws + OFF_WA, nullptr, qb, kvb);
    attn_kernel<<<256, 512, 0, stream>>>(qb, kvb, obuf);
    gemm_kernel<1><<<192, 768, 0, stream>>>(obuf, ws + OFF_WOA, xin, xmid, nullptr);
    gemm_kernel<2><<<256, 896, 0, stream>>>(xmid, ws + OFF_WB, nullptr, obuf, nullptr);
    gemm_kernel<1><<<192, 768, 0, stream>>>(obuf, ws + OFF_WOB, xmid, xout, nullptr);
  }
}
```

```cpp
#include <hip/hip_runtime.h>
#include <hip/hip_bf16.h>
#include <stdint.h>

typedef __attribute__((ext_vector_type(8))) short bf16x8;
typedef __attribute__((ext_vector_type(4))) float f32x4;
typedef __attribute__((ext_vector_type(2))) float f32x2;
typedef __attribute__((ext_vector_type(16))) float f32x16;
typedef __attribute__((ext_vector_type(4))) unsigned u32x4;
typedef __attribute__((ext_vector_type(4), aligned(4))) unsigned u32x4_a4;
typedef __attribute__((ext_vector_type(2))) unsigned u32x2;
typedef __attribute__((ext_vector_type(2))) __bf16 bf16x2_t;
typedef unsigned short u16;

#define DI __device__ __forceinline__

constexpr int SEQ = 2048;
constexpr int DM = 576;
constexpr int FRAG_TILE = 18 * 1024;

constexpr int REC = 2368;
constexpr int REC_K1 = 1024;
constexpr int REC_V = 1152;
constexpr int KV_BYTES = 64 * REC;
constexpr int CHUNK = 16 * REC;

constexpr size_t OFF_WA = 0;
constexpr size_t OFF_WOA = OFF_WA + 39 * (size_t)FRAG_TILE;
constexpr size_t OFF_WB = OFF_WOA + 36 * (size_t)FRAG_TILE;
constexpr size_t OFF_WOB = OFF_WB + 112 * (size_t)FRAG_TILE;
constexpr size_t ACT_BYTES = (size_t)SEQ * DM * 2;
constexpr size_t OFF_Q = OFF_WOB + 36 * (size_t)FRAG_TILE;
constexpr size_t OFF_KV = OFF_Q + ACT_BYTES;
constexpr size_t OFF_O = OFF_KV + KV_BYTES;
constexpr size_t OFF_X1 = OFF_O + ACT_BYTES;
constexpr size_t OFF_X2 = OFF_X1 + ACT_BYTES;
constexpr size_t OFF_X3 = OFF_X2 + ACT_BYTES;

DI float bf_lo(unsigned u) { return __uint_as_float(u << 16); }
DI float bf_hi(unsigned u) { return __uint_as_float(u & 0xffff0000u); }
DI unsigned pack_bf16(float a, float b) {
  f32x2 v = {a, b};
  bf16x2_t r = __builtin_convertvector(v, bf16x2_t);
  return __builtin_bit_cast(unsigned, r);
}
DI float round_bf16(float a) { return bf_lo(pack_bf16(a, 0.f)); }
template <int CTRL>
DI float dpp_f(float v) {
  return __builtin_bit_cast(float, __builtin_amdgcn_update_dpp(0, __builtin_bit_cast(int, v), CTRL, 0xf, 0xf, false));
}
DI float red8(float v) {
  v += dpp_f<0xB1>(v);
  v += dpp_f<0x4E>(v);
  v += dpp_f<0x141>(v);
  return v;
}
DI float dot2_bf16(unsigned a, unsigned b, float c) {
  return __builtin_amdgcn_fdot2_f32_bf16(__builtin_bit_cast(bf16x2_t, a), __builtin_bit_cast(bf16x2_t, b), c, false);
}

constexpr int PREP_FAST = 55 * 18;
constexpr int PREP_WAVES = PREP_FAST + 3 * 18;
__global__ __launch_bounds__(256) void prep_kernel(const u16* __restrict__ wq, const u16* __restrict__ wk,
                                                   const u16* __restrict__ wv, const u16* __restrict__ woa,
                                                   const u16* __restrict__ wqkv, const u16* __restrict__ wob,
                                                   char* __restrict__ ws) {
  const int wid = blockIdx.x * 4 + (threadIdx.x >> 6);
  const int lane = threadIdx.x & 63;
  if (wid >= PREP_WAVES) return;
  const int g = lane >> 4, c = lane & 15;
  if (wid < PREP_FAST) {
    const int grp = wid / 18, s = wid % 18;
    const u16* src;
    int ld = 576;
    bool zero = false;
    char* dst;
    if (grp < 9) {
      dst = ws + OFF_WA + (size_t)grp * 4 * FRAG_TILE;
      src = wq + grp * 64 + c * 4;
    } else if (grp < 18) {
      dst = ws + OFF_WOA + (size_t)(grp - 9) * 4 * FRAG_TILE;
      src = woa + (grp - 9) * 64 + c * 4;
    } else if (grp < 27) {
      dst = ws + OFF_WOB + (size_t)(grp - 18) * 4 * FRAG_TILE;
      src = wob + (grp - 18) * 64 + c * 4;
    } else {
      dst = ws + OFF_WB + (size_t)(grp - 27) * 4 * FRAG_TILE;
      const int np = (grp - 27) * 64 + c * 4;
      const int hg = np / 224, n = np % 224;
      ld = 1728;
      zero = n >= 216;
      const int nn = zero ? 0 : n;
      src = wqkv + (nn / 72) * 576 + hg * 72 + (nn % 72);
    }
    u32x2 r[8];
    const int k0 = 32 * s + 8 * g;
#pragma unroll
    for (int j = 0; j < 8; ++j) r[j] = *(const u32x2*)(src + (size_t)(k0 + j) * ld);
    if (zero) {
#pragma unroll
      for (int j = 0; j < 8; ++j) r[j] = u32x2{0u, 0u};
    }
    char* dp = dst + (size_t)(c >> 2) * FRAG_TILE + s * 1024 + (g * 16 + (c & 3) * 4) * 16;
#pragma unroll
    for (int q = 0; q < 4; ++q) {
      u32x4 v;
#pragma unroll
      for (int j = 0; j < 4; ++j)
        v[j] = __builtin_amdgcn_perm(r[2 * j + 1][q >> 1], r[2 * j][q >> 1], (q & 1) ? 0x07060302u : 0x05040100u);
      *(u32x4*)(dp + q * 16) = v;
    }
  } else {
    const int w2 = wid - PREP_FAST;
    const int tile = 36 + w2 / 18, s = w2 % 18;
    const int n = tile * 16 + c;
    const u16* src = (n < 594) ? wk + (n - 576) : wv + (n < 612 ? n - 594 : 0);
    const int k0 = 32 * s + 8 * g;
    u32x4 v;
#pragma unroll
    for (int j = 0; j < 4; ++j) {
      unsigned lo = src[(size_t)(k0 + 2 * j) * 18];
      unsigned hi = src[(size_t)(k0 + 2 * j + 1) * 18];
      v[j] = n < 612 ? (lo | (hi << 16)) : 0u;
    }
    *(u32x4*)(ws + OFF_WA + (size_t)tile * FRAG_TILE + s * 1024 + lane * 16) = v;
  }
}

template <int MODE>
struct GemmCfg {
  static constexpr int ROWS = (MODE == 2) ? 80 : 64;
  static constexpr int RT = ROWS / 16;
  static constexpr int NT = (MODE == 2) ? 14 : 6;
  static constexpr int KS = (MODE == 2) ? 1 : 2;
  static constexpr int NW = NT * KS;
  static constexpr int KSTEPS = 18 / KS;
  static constexpr int PF = (MODE == 2) ? 5 : 4;
  static constexpr int CB = (MODE == 0) ? 7 : (MODE == 1 ? 6 : 8);
  static constexpr int NACC = RT + (MODE == 1 ? 0 : 1);
  static constexpr int OFF_STAT = ROWS * 1152;
  static constexpr int OFF_RED = OFF_STAT + ROWS * 8;
  static constexpr int SMEM = OFF_RED + (KS == 2 ? NT * NACC * 1024 : 0);
};

template <int MODE>
__global__ __launch_bounds__(GemmCfg<MODE>::NW * 64) void gemm_kernel(const u16* __restrict__ A, const char* __restrict__ Wf,
                                                                      const u16* __restrict__ resid, u16* __restrict__ out,
                                                                      char* __restrict__ kvout) {
  using C = GemmCfg<MODE>;
  __shared__ __attribute__((aligned(16))) char smem[C::SMEM];
  const int tid = threadIdx.x, lane = tid & 63, wave = tid >> 6;
  const int xcd = blockIdx.x & 7, idx = blockIdx.x >> 3;
  const int rb = xcd * 4 + idx / C::CB, cb = idx % C::CB;
  const int tcol = wave % C::NT, kh = wave / C::NT;
  const int gtile = cb * C::NT + tcol;
  const bool active = (MODE != 0) || (gtile < 39);
  const int row0 = rb * 64 - (MODE == 2 ? 16 : 0);
  const int mloc = lane & 15, g = lane >> 4, nq = g * 4;

  const int sr = wave * 8 + (lane >> 3), sj = lane & 7;
  u32x4 xa[9];
  if (wave < C::ROWS / 8) {
    int grow = row0 + sr;
    if (MODE == 2) grow = grow < 0 ? 0 : grow;
    const u16* ap = A + (size_t)grow * DM + sj * 8;
#pragma unroll
    for (int i = 0; i < 9; ++i) xa[i] = *(const u32x4*)(ap + i * 64);
  }
  __builtin_amdgcn_sched_barrier(0);
  __builtin_amdgcn_s_barrier();
  __builtin_amdgcn_sched_barrier(0);
  bf16x8 wf[C::KSTEPS];
  const char* wp = Wf + (size_t)(active ? gtile : 0) * FRAG_TILE + (size_t)kh * C::KSTEPS * 1024 + lane * 16;
#pragma unroll
  for (int s = 0; s < C::PF; ++s) wf[s] = *(const bf16x8*)(wp + s * 1024);
  __builtin_amdgcn_sched_barrier(0);
  if (wave < C::ROWS / 8) {
    char* lp = smem + sr * 1152 + ((sj ^ ((sr >> 1) & 7)) * 16);
#pragma unroll
    for (int i = 0; i < 9; ++i) *(u32x4*)(lp + i * 128) = xa[i];
  }
  __syncthreads();
  if (MODE != 1 && wave < C::ROWS / 8) {
    float sum = 0.f, sq = 0.f;
#pragma unroll
    for (int i = 0; i < 9; ++i)
#pragma unroll
      for (int j = 0; j < 4; ++j) {
        sum = dot2_bf16(xa[i][j], 0x3f803f80u, sum);
        sq = dot2_bf16(xa[i][j], xa[i][j], sq);
      }
    sum = red8(sum);
    sq = red8(sq);
    const float mean = sum * (1.f / 576.f);
    float var = sq * (1.f / 576.f) - mean * mean;
    if (__any(!(mean * mean <= 16.f * var))) {
      var = 0.f;
#pragma unroll
      for (int i = 0; i < 9; ++i)
#pragma unroll
        for (int j = 0; j < 4; ++j) {
          float a = bf_lo(xa[i][j]) - mean, b = bf_hi(xa[i][j]) - mean;
          var += a * a + b * b;
        }
      var = red8(var) * (1.f / 576.f);
    }
    const float rstd = 1.0f / sqrtf(var + 1e-5f);
    if (sj == 0) *(f32x2*)(smem + C::OFF_STAT + sr * 8) = f32x2{mean, rstd};
  }
  __builtin_amdgcn_sched_barrier(0);

  f32x4 acc[C::NACC];
#pragma unroll
  for (int t = 0; t < C::NACC; ++t) acc[t] = f32x4{0.f, 0.f, 0.f, 0.f};
  u32x2 xr[C::RT];
  {
    const int x = mloc >> 1;
    const int be = (g ^ x) * 16, bo = be ^ 64;
    const char* pA = smem + mloc * 1152 + (kh ? bo + 512 : be);
    const char* pB = smem + mloc * 1152 + (kh ? be + 640 : bo);
    const bf16x8 ones = {0x3f80, 0x3f80, 0x3f80, 0x3f80, 0x3f80, 0x3f80, 0x3f80, 0x3f80};
#pragma unroll
    for (int s = 0; s < C::KSTEPS; ++s) {
      if (s + C::PF < C::KSTEPS) wf[s + C::PF] = *(const bf16x8*)(wp + (s + C::PF) * 1024);
      if (MODE == 1 && s + C::PF == C::KSTEPS && kh == 0) {
#pragma unroll
        for (int t = 0; t < C::RT; ++t) xr[t] = *(const u32x2*)(resid + (size_t)(row0 + t * 16 + mloc) * DM + gtile * 16 + nq);
      }
      const char* ap = ((s & 1) ? pB : pA) + (s >> 1) * 128;
#pragma unroll
      for (int t = 0; t < C::RT; ++t) {
        bf16x8 af = *(const bf16x8*)(ap + t * 16 * 1152);
        acc[t] = __builtin_amdgcn_mfma_f32_16x16x32_bf16(wf[s], af, acc[t], 0, 0, 0);
      }
      if (MODE != 1) acc[C::RT] = __builtin_amdgcn_mfma_f32_16x16x32_bf16(wf[s], ones, acc[C::RT], 0, 0, 0);
      __builtin_amdgcn_sched_barrier(0);
    }
  }
  if (C::KS == 2) {
    char* red = smem + C::OFF_RED + tcol * C::NACC * 1024 + lane * 16;
    if (kh == 1) {
#pragma unroll
      for (int t = 0; t < C::NACC; ++t) *(f32x4*)(red + t * 1024) = acc[t];
    }
    __syncthreads();
    if (kh == 1) return;
#pragma unroll
    for (int t = 0; t < C::NACC; ++t) acc[t] += *(const f32x4*)(red + t * 1024);
  } else {
    __syncthreads();
  }
  if (MODE != 1) {
#pragma unroll
    for (int t = 0; t < C::RT; ++t) {
      const f32x2 st = *(const f32x2*)(smem + C::OFF_STAT + (t * 16 + mloc) * 8);
#pragma unroll
      for (int i = 0; i < 4; ++i) acc[t][i] = (acc[t][i] - st[0] * acc[C::RT][i]) * st[1];
    }
  }

  if (MODE == 1) {
    const int n = gtile * 16 + nq;
#pragma unroll
    for (int t = 0; t < C::RT; ++t) {
      const size_t off = (size_t)(row0 + t * 16 + mloc) * DM + n;
      float r0 = round_bf16(acc[t][0]), r1 = round_bf16(acc[t][1]), r2 = round_bf16(acc[t][2]), r3 = round_bf16(acc[t][3]);
      u32x2 o;
      o[0] = pack_bf16(bf_lo(xr[t][0]) + r0, bf_hi(xr[t][0]) + r1);
      o[1] = pack_bf16(bf_lo(xr[t][1]) + r2, bf_hi(xr[t][1]) + r3);
      *(u32x2*)(out + off) = o;
    }
  } else if (MODE == 0) {
    if (!active) return;
    const float qs = 0.23570226039551584f * 1.4426950408889634f;
    if (gtile < 36) {
#pragma unroll
      for (int t = 0; t < C::RT; ++t) {
        const int pos = row0 + t * 16 + mloc;
        u32x2 o;
        o[0] = pack_bf16(acc[t][0] * qs, acc[t][1] * qs);
        o[1] = pack_bf16(acc[t][2] * qs, acc[t][3] * qs);
        *(u32x2*)(out + (size_t)pos * DM + gtile * 16 + nq) = o;
      }
    } else {
#pragma unroll
      for (int t = 0; t < C::RT; ++t) {
        const int pos = row0 + t * 16 + mloc;
        const int r = pos & 31;
        char* rec = kvout + (pos >> 5) * REC;
        const unsigned w0 = pack_bf16(acc[t][0], acc[t][1]), w1 = pack_bf16(acc[t][2], acc[t][3]);
        if (gtile == 36) {
          *(u32x2*)(rec + (g >> 1) * 512 + r * 16 + (g & 1) * 8) = u32x2{w0, w1};
        } else {
          const int s = r >> 4, rr = r & 15;
          const int hh = (rr >> 2) & 1, j = ((rr >> 3) << 2) | (rr & 3);
          char* vb = rec + REC_V + s * 608 + hh * 304 + j * 2;
          const int d0 = (gtile - 37) * 16 + nq - 2;
          if (d0 < 0) *(unsigned*)(rec + REC_K1 + r * 4) = w0;
          else if (d0 < 18) {
            *(u16*)(vb + d0 * 16) = (u16)(w0 & 0xffffu);
            *(u16*)(vb + (d0 + 1) * 16) = (u16)(w0 >> 16);
          } else if (d0 == 18) *(u16*)(vb + 18 * 16) = (u16)0x3f80;
          if (d0 + 2 < 18) {
            *(u16*)(vb + (d0 + 2) * 16) = (u16)(w1 & 0xffffu);
            *(u16*)(vb + (d0 + 3) * 16) = (u16)(w1 >> 16);
          }
        }
      }
    }
  } else {
    constexpr int LDQ = 456;
#pragma unroll
    for (int t = 0; t < C::RT; ++t) {
      u32x2 o;
      o[0] = pack_bf16(acc[t][0], acc[t][1]);
      o[1] = pack_bf16(acc[t][2], acc[t][3]);
      *(u32x2*)(smem + (t * 16 + mloc) * LDQ + (tcol * 16 + nq) * 2) = o;
    }
    __syncthreads();
    if (tid < 512) {
      const int half = tid & 1, hh = ((tid >> 1) & 1) | ((tid >> 4) & 2), i = ((tid >> 2) & 7) | ((tid >> 6) << 3);
      const int pos = rb * 64 + i;
      const char* qrow = smem + (16 + i) * LDQ + hh * 36;
      unsigned q[9];
#pragma unroll
      for (int d = 0; d < 9; ++d) q[d] = *(const unsigned*)(qrow + d * 4);
      const char* kbase = qrow - 8 * half * LDQ;
      float lg[4];
      float mx = -3.0e38f;
#pragma unroll
      for (int w = 0; w < 4; ++w) {
        const char* krow = kbase - 2 * w * LDQ + 144;
        float sacc = 0.f;
#pragma unroll
        for (int d = 0; d < 9; ++d) sacc = dot2_bf16(q[d], *(const unsigned*)(krow + d * 4), sacc);
        sacc *= 0.23570226039551584f;
        lg[w] = (pos - 2 * (w + 4 * half) >= 0) ? sacc : -1e9f;
        mx = fmaxf(mx, lg[w]);
      }
      mx = fmaxf(mx, dpp_f<0xB1>(mx));
      float den = 0.f;
#pragma unroll
      for (int w = 0; w < 4; ++w) {
        lg[w] = __expf(lg[w] - mx);
        den += lg[w];
      }
      den += dpp_f<0xB1>(den);
      const float inv = 1.f / den;
      float o[18];
#pragma unroll
      for (int d = 0; d < 18; ++d) o[d] = 0.f;
#pragma unroll
      for (int w = 0; w < 4; ++w) {
        const char* vrow = kbase - 2 * w * LDQ + 288;
        const float p = lg[w] * inv;
#pragma unroll
        for (int d = 0; d < 9; ++d) {
          unsigned u = *(const unsigned*)(vrow + d * 4);
          o[2 * d] += p * bf_lo(u);
          o[2 * d + 1] += p * bf_hi(u);
        }
      }
#pragma unroll
      for (int d = 0; d < 18; ++d) o[d] += dpp_f<0xB1>(o[d]);
      unsigned* op = (unsigned*)(out + (size_t)pos * DM + (cb * 4 + hh) * 18);
      if (half == 0) {
#pragma unroll
        for (int d = 0; d < 5; ++d) op[d] = pack_bf16(o[2 * d], o[2 * d + 1]);
      } else {
#pragma unroll
        for (int d = 5; d < 9; ++d) op[d] = pack_bf16(o[2 * d], o[2 * d + 1]);
      }
    }
  }
}

#define MFMA32(a, b, c) __builtin_amdgcn_mfma_f32_32x32x16_bf16(a, b, c, 0, 0, 0)
#define EXP2(x) __builtin_amdgcn_exp2f(x)

__global__ __launch_bounds__(512) void attn_kernel(const u16* __restrict__ qin, const char* __restrict__ kvin,
                                                   u16* __restrict__ out) {
  __shared__ __attribute__((aligned(16))) char smem[66 * REC];
  const int tid = threadIdx.x, lane = tid & 63, wave = tid >> 6;
  const int r = lane & 31, h = lane >> 5;
  u32x4 st[5];
  int soff[5];
#pragma unroll
  for (int j = 0; j < 5; ++j) {
    int i = wave + 8 * j;
    soff[j] = (i > 36 ? 36 : i) * 1024 + lane * 16;
  }
#pragma unroll
  for (int j = 0; j < 5; ++j) st[j] = *(const u32x4*)(kvin + soff[j]);
  const int pos = blockIdx.x * 8 + wave;
  const u16* qp = qin + (size_t)pos * DM + r * 18;
  u32x4 q0u = *(const u32x4_a4*)(qp + h * 8);
  unsigned q1w = *(const unsigned*)(qp + 16);
  if (h) q1w = 0u;
  const bf16x8 q0 = __builtin_bit_cast(bf16x8, q0u);
  const bf16x8 q1 = __builtin_bit_cast(bf16x8, u32x4{q1w, 0u, 0u, 0u});
#pragma unroll
  for (int j = 0; j < 5; ++j) *(u32x4*)(smem + soff[j]) = st[j];
#pragma unroll
  for (int j = 0; j < 5; ++j) st[j] = *(const u32x4*)(kvin + CHUNK + soff[j]);
  __syncthreads();

  const char* k0p = smem + h * 512 + r * 16;
  const char* k1p = smem + REC_K1 + r * 4;
  const char* vp = smem + REC_V + h * 304 + (r < 18 ? r : 18) * 16;

  f32x16 zero;
#pragma unroll
  for (int i = 0; i < 16; ++i) zero[i] = 0.f;
  f32x16 acc = zero;
  f32x16 negm, sA, sB;
  {
    bf16x8 k0 = *(const bf16x8*)(k0p);
    bf16x8 k1 = __builtin_bit_cast(bf16x8, u32x4{*(const unsigned*)(k1p), 0u, 0u, 0u});
    f32x16 s = MFMA32(k0, q0, zero);
    s = MFMA32(k1, q1, s);
    float mx = s[0];
#pragma unroll
    for (int i = 1; i < 16; ++i) mx = fmaxf(mx, s[i]);
    mx = fmaxf(mx, __shfl_xor(mx, 32));
#pragma unroll
    for (int i = 0; i < 16; ++i) negm[i] = -mx;
#pragma unroll
    for (int i = 0; i < 16; ++i) sA[i] = s[i] - mx;
  }
  bf16x8 k0A = *(const bf16x8*)(k0p + REC), k0B;
  u32x4 k1A = {*(const unsigned*)(k1p + REC), 0u, 0u, 0u}, k1B = {0u, 0u, 0u, 0u};
  bf16x8 v0A = {0, 0, 0, 0, 0, 0, 0, 0}, v1A = v0A, v0B, v1B;
  u32x4 pA0 = {0u, 0u, 0u, 0u}, pA1 = pA0, pB0, pB1;

#define SB __builtin_amdgcn_sched_barrier(0);
#define TILE_BODY(OFF, SC, SN, K0C, K1C, V0C, V1C, K0N, K1N, V0N, V1N, PP0, PP1, PC0, PC1)                     \
  {                                                                                                            \
    acc = MFMA32(V0C, __builtin_bit_cast(bf16x8, PP0), acc);                                                   \
    K0N = *(const bf16x8*)(k0p + (OFF) + 2 * REC);                                                             \
    K1N[0] = *(const unsigned*)(k1p + (OFF) + 2 * REC);                                                        \
    SB                                                                                                         \
    PC0[0] = pack_bf16(EXP2(SC[0]), EXP2(SC[1]));                                                              \
    PC0[1] = pack_bf16(EXP2(SC[2]), EXP2(SC[3]));                                                              \
    SB                                                                                                         \
    acc = MFMA32(V1C, __builtin_bit_cast(bf16x8, PP1), acc);                                                   \
    V0N = *(const bf16x8*)(vp + (OFF));                                                                        \
    V1N = *(const bf16x8*)(vp + (OFF) + 608);                                                                  \
    SB                                                                                                         \
    PC0[2] = pack_bf16(EXP2(SC[4]), EXP2(SC[5]));                                                              \
    PC0[3] = pack_bf16(EXP2(SC[6]), EXP2(SC[7]));                                                              \
    SB                                                                                                         \
    SN = MFMA32(K0C, q0, negm);                                                                                \
    SB                                                                                                         \
    PC1[0] = pack_bf16(EXP2(SC[8]), EXP2(SC[9]));                                                              \
    PC1[1] = pack_bf16(EXP2(SC[10]), EXP2(SC[11]));                                                            \
    SB                                                                                                         \
    SN = MFMA32(__builtin_bit_cast(bf16x8, K1C), q1, SN);                                                        \
    SB                                                                                                         \
    PC1[2] = pack_bf16(EXP2(SC[12]), EXP2(SC[13]));                                                            \
    PC1[3] = pack_bf16(EXP2(SC[14]), EXP2(SC[15]));                                                            \
    SB                                                                                                         \
  }

  for (int t = 0; t < 64; t += 2) {
    if ((t & 15) == 8 && t < 48) {
      const int c = (t >> 4) + 1;
#pragma unroll
      for (int j = 0; j < 5; ++j) *(u32x4*)(smem + c * CHUNK + soff[j]) = st[j];
      if (c < 3) {
#pragma unroll
        for (int j = 0; j < 5; ++j) st[j] = *(const u32x4*)(kvin + (c + 1) * CHUNK + soff[j]);
      }
      asm volatile("s_waitcnt lgkmcnt(0)" ::: "memory");
      __builtin_amdgcn_s_barrier();
      asm volatile("" ::: "memory");
    }
    const int off = t * REC;
    TILE_BODY(off, sA, sB, k0A, k1A, v0A, v1A, k0B, k1B, v0B, v1B, pA0, pA1, pB0, pB1)
    TILE_BODY(off + REC, sB, sA, k0B, k1B, v0B, v1B, k0A, k1A, v0A, v1A, pB0, pB1, pA0, pA1)
  }
  acc = MFMA32(v0A, __builtin_bit_cast(bf16x8, pA0), acc);
  acc = MFMA32(v1A, __builtin_bit_cast(bf16x8, pA1), acc);

  {
    bool bad = !(acc[10] < 1.0e30f);
#pragma unroll
    for (int i = 0; i < 10; ++i) bad = bad || !(fabsf(acc[i]) < 3.0e38f);
    if (__any(bad)) {
      acc = zero;
      float m = -3.0e38f;
      for (int t = 0; t < 64; ++t) {
        bf16x8 k0 = *(const bf16x8*)(k0p + t * REC);
        bf16x8 k1 = __builtin_bit_cast(bf16x8, u32x4{*(const unsigned*)(k1p + t * REC), 0u, 0u, 0u});
        bf16x8 v0 = *(const bf16x8*)(vp + t * REC);
        bf16x8 v1 = *(const bf16x8*)(vp + t * REC + 608);
        f32x16 s = MFMA32(k0, q0, zero);
        s = MFMA32(k1, q1, s);
        float mx = s[0];
#pragma unroll
        for (int i = 1; i < 16; ++i) mx = fmaxf(mx, s[i]);
        mx = fmaxf(mx, __shfl_xor(mx, 32));
        const float mnew = fmaxf(m, mx);
        const float sc = EXP2(m - mnew);
        m = mnew;
#pragma unroll
        for (int i = 0; i < 16; ++i) acc[i] *= sc;
        u32x4 p0, p1;
#pragma unroll
        for (int i = 0; i < 4; ++i) {
          p0[i] = pack_bf16(EXP2(s[2 * i] - m), EXP2(s[2 * i + 1] - m));
          p1[i] = pack_bf16(EXP2(s[8 + 2 * i] - m), EXP2(s[8 + 2 * i + 1] - m));
        }
        acc = MFMA32(v0, __builtin_bit_cast(bf16x8, p0), acc);
        acc = MFMA32(v1, __builtin_bit_cast(bf16x8, p1), acc);
      }
    }
  }
  const float inv = 1.f / acc[10];
  u16* op = out + (size_t)pos * DM + r * 18;
  {
    unsigned a0 = pack_bf16(acc[0] * inv, acc[1] * inv), a1 = pack_bf16(acc[2] * inv, acc[3] * inv);
    unsigned b0 = pack_bf16(acc[4] * inv, acc[5] * inv), b1 = pack_bf16(acc[6] * inv, acc[7] * inv);
    unsigned* o32 = (unsigned*)(op + h * 4);
    o32[0] = a0;
    o32[1] = a1;
    o32[4] = b0;
    o32[5] = b1;
    if (!h) *(unsigned*)(op + 16) = pack_bf16(acc[8] * inv, acc[9] * inv);
  }
}

extern "C" void kernel_launch(void* const* d_in, const int* in_sizes, int n_in, void* d_out, int out_size, void* d_ws,
                              size_t ws_size, hipStream_t stream) {
  const u16* x0 = (const u16*)d_in[0];
  const u16* wq = (const u16*)d_in[1];
  const u16* wk = (const u16*)d_in[2];
  const u16* wv = (const u16*)d_in[3];
  const u16* woa = (const u16*)d_in[4];
  const u16* wqkv = (const u16*)d_in[6];
  const u16* wob = (const u16*)d_in[7];
  char* ws = (char*)d_ws;
  u16* obuf = (u16*)(ws + OFF_O);
  u16* x1 = (u16*)(ws + OFF_X1);
  u16* x2 = (u16*)(ws + OFF_X2);
  u16* x3 = (u16*)(ws + OFF_X3);
  u16* qb = (u16*)(ws + OFF_Q);
  char* kvb = ws + OFF_KV;

  prep_kernel<<<(PREP_WAVES + 3) / 4, 256, 0, stream>>>(wq, wk, wv, woa, wqkv, wob, ws);
  for (int layer = 0; layer < 2; ++layer) {
    const u16* xin = layer == 0 ? x0 : x2;
    u16* xmid = layer == 0 ? x1 : x3;
    u16* xout = layer == 0 ? x2 : (u16*)d_out;
    gemm_kernel<0><<<224, 768, 0, stream>>>(xin, ws + OFF_WA, nullptr, qb, kvb);
    attn_kernel<<<256, 512, 0, stream>>>(qb, kvb, obuf);
    gemm_kernel<1><<<192, 768, 0, stream>>>(obuf, ws + OFF_WOA, xin, xmid, nullptr);
    gemm_kernel<2><<<256, 896, 0, stream>>>(xmid, ws + OFF_WB, nullptr, obuf, nullptr);
    gemm_kernel<1><<<192, 768, 0, stream>>>(obuf, ws + OFF_WOB, xmid, xout, nullptr);
  }
}
```

```cpp
#include <hip/hip_runtime.h>
#include <hip/hip_bf16.h>
#include <stdint.h>
#include <type_traits>

typedef __attribute__((ext_vector_type(8))) short bf16x8;
typedef __attribute__((ext_vector_type(4))) float f32x4;
typedef __attribute__((ext_vector_type(2))) float f32x2;
typedef __attribute__((ext_vector_type(16))) float f32x16;
typedef __attribute__((ext_vector_type(4))) unsigned u32x4;
typedef __attribute__((ext_vector_type(4), aligned(4))) unsigned u32x4_a4;
typedef __attribute__((ext_vector_type(2))) unsigned u32x2;
typedef __attribute__((ext_vector_type(2))) __bf16 bf16x2_t;
typedef unsigned short u16;

#define DI __device__ __forceinline__

constexpr int SEQ = 2048;
constexpr int DM = 576;
constexpr int FRAG_TILE = 18 * 1024;

constexpr int REC = 2368;
constexpr int REC_K1 = 1024;
constexpr int REC_V = 1152;
constexpr int KV_BYTES = 64 * REC;
constexpr int CHUNK = 16 * REC;

constexpr size_t OFF_WA = 0;
constexpr size_t OFF_WOA = OFF_WA + 39 * (size_t)FRAG_TILE;
constexpr size_t OFF_WB = OFF_WOA + 36 * (size_t)FRAG_TILE;
constexpr size_t OFF_WOB = OFF_WB + 112 * (size_t)FRAG_TILE;
constexpr size_t ACT_BYTES = (size_t)SEQ * DM * 2;
constexpr size_t OFF_Q = OFF_WOB + 36 * (size_t)FRAG_TILE;
constexpr size_t OFF_KV = OFF_Q + ACT_BYTES;
constexpr size_t OFF_O = OFF_KV + KV_BYTES;
constexpr size_t OFF_X1 = OFF_O + ACT_BYTES;
constexpr size_t OFF_X2 = OFF_X1 + ACT_BYTES;
constexpr size_t OFF_X3 = OFF_X2 + ACT_BYTES;
constexpr int NCA = 39 * 16, NCB = 112 * 16;
constexpr size_t OFF_CSA = OFF_X3 + ACT_BYTES;
constexpr size_t OFF_CSB = OFF_CSA + 18 * (size_t)NCA * 4;

DI float bf_lo(unsigned u) { return __uint_as_float(u << 16); }
DI float bf_hi(unsigned u) { return __uint_as_float(u & 0xffff0000u); }
DI unsigned pack_bf16(float a, float b) {
  f32x2 v = {a, b};
  bf16x2_t r = __builtin_convertvector(v, bf16x2_t);
  return __builtin_bit_cast(unsigned, r);
}
DI float round_bf16(float a) { return bf_lo(pack_bf16(a, 0.f)); }
template <int CTRL>
DI float dpp_f(float v) {
  return __builtin_bit_cast(float, __builtin_amdgcn_update_dpp(0, __builtin_bit_cast(int, v), CTRL, 0xf, 0xf, false));
}
DI float red8(float v) {
  v += dpp_f<0xB1>(v);
  v += dpp_f<0x4E>(v);
  v += dpp_f<0x141>(v);
  return v;
}
DI float dot2_bf16(unsigned a, unsigned b, float c) {
  return __builtin_amdgcn_fdot2_f32_bf16(__builtin_bit_cast(bf16x2_t, a), __builtin_bit_cast(bf16x2_t, b), c, false);
}

constexpr int PREP_FAST = 55 * 18;
constexpr int PREP_WAVES = PREP_FAST + 3 * 18;
__global__ __launch_bounds__(256) void prep_kernel(const u16* __restrict__ wq, const u16* __restrict__ wk,
                                                   const u16* __restrict__ wv, const u16* __restrict__ woa,
                                                   const u16* __restrict__ wqkv, const u16* __restrict__ wob,
                                                   char* __restrict__ ws) {
  const int wid = blockIdx.x * 4 + (threadIdx.x >> 6);
  const int lane = threadIdx.x & 63;
  if (wid >= PREP_WAVES) return;
  const int g = lane >> 4, c = lane & 15;
  if (wid < PREP_FAST) {
    const int grp = wid / 18, s = wid % 18;
    const u16* src;
    int ld = 576;
    bool zero = false;
    char* dst;
    float* cs = nullptr;
    if (grp < 9) {
      dst = ws + OFF_WA + (size_t)grp * 4 * FRAG_TILE;
      cs = (float*)(ws + OFF_CSA) + s * NCA + grp * 64 + c * 4;
      src = wq + grp * 64 + c * 4;
    } else if (grp < 18) {
      dst = ws + OFF_WOA + (size_t)(grp - 9) * 4 * FRAG_TILE;
      src = woa + (grp - 9) * 64 + c * 4;
    } else if (grp < 27) {
      dst = ws + OFF_WOB + (size_t)(grp - 18) * 4 * FRAG_TILE;
      src = wob + (grp - 18) * 64 + c * 4;
    } else {
      dst = ws + OFF_WB + (size_t)(grp - 27) * 4 * FRAG_TILE;
      const int np = (grp - 27) * 64 + c * 4;
      cs = (float*)(ws + OFF_CSB) + s * NCB + np;
      const int hg = np / 224, n = np % 224;
      ld = 1728;
      zero = n >= 216;
      const int nn = zero ? 0 : n;
      src = wqkv + (nn / 72) * 576 + hg * 72 + (nn % 72);
    }
    u32x2 r[8];
    const int k0 = 32 * s + 8 * g;
#pragma unroll
    for (int j = 0; j < 8; ++j) r[j] = *(const u32x2*)(src + (size_t)(k0 + j) * ld);
    if (zero) {
#pragma unroll
      for (int j = 0; j < 8; ++j) r[j] = u32x2{0u, 0u};
    }
    char* dp = dst + (size_t)(c >> 2) * FRAG_TILE + s * 1024 + (g * 16 + (c & 3) * 4) * 16;
    f32x4 csum;
#pragma unroll
    for (int q = 0; q < 4; ++q) {
      u32x4 v;
      float a = 0.f;
#pragma unroll
      for (int j = 0; j < 4; ++j) {
        v[j] = __builtin_amdgcn_perm(r[2 * j + 1][q >> 1], r[2 * j][q >> 1], (q & 1) ? 0x07060302u : 0x05040100u);
        a = dot2_bf16(v[j], 0x3f803f80u, a);
      }
      *(u32x4*)(dp + q * 16) = v;
      a += __shfl_xor(a, 16);
      a += __shfl_xor(a, 32);
      csum[q] = a;
    }
    if (cs != nullptr && g == 0) *(f32x4*)cs = csum;
  } else {
    const int w2 = wid - PREP_FAST;
    const int tile = 36 + w2 / 18, s = w2 % 18;
    const int n = tile * 16 + c;
    const u16* src = (n < 594) ? wk + (n - 576) : wv + (n < 612 ? n - 594 : 0);
    const int k0 = 32 * s + 8 * g;
    u32x4 v;
    float a = 0.f;
#pragma unroll
    for (int j = 0; j < 4; ++j) {
      unsigned lo = src[(size_t)(k0 + 2 * j) * 18];
      unsigned hi = src[(size_t)(k0 + 2 * j + 1) * 18];
      v[j] = n < 612 ? (lo | (hi << 16)) : 0u;
      a = dot2_bf16(v[j], 0x3f803f80u, a);
    }
    a += __shfl_xor(a, 16);
    a += __shfl_xor(a, 32);
    if (g == 0) ((float*)(ws + OFF_CSA))[s * NCA + n] = a;
    *(u32x4*)(ws + OFF_WA + (size_t)tile * FRAG_TILE + s * 1024 + lane * 16) = v;
  }
}

template <int MODE>
struct GemmCfg {
  static constexpr int ROWS = (MODE == 2) ? 80 : 64;
  static constexpr int RT = ROWS / 16;
  static constexpr int NT = (MODE == 2) ? 14 : 6;
  static constexpr int KS = (MODE == 2) ? 1 : 2;
  static constexpr int NW = NT * KS;
  static constexpr int KSTEPS = 18 / KS;
  static constexpr int PF = (MODE == 2) ? 5 : 4;
  static constexpr int CB = (MODE == 0) ? 7 : (MODE == 1 ? 6 : 8);
  static constexpr int NACC = RT;
  static constexpr int SPC = KSTEPS / 3;
  static constexpr int NC = (MODE == 2) ? NCB : NCA;
  static constexpr int OFF_STAT = ROWS * 1152;
  static constexpr int OFF_RED = OFF_STAT + ROWS * 8;
  static constexpr int SMEM = OFF_RED + (KS == 2 ? NT * NACC * 1024 : 0);
};

template <int MODE>
__global__ __launch_bounds__(GemmCfg<MODE>::NW * 64) void gemm_kernel(const u16* __restrict__ A, const char* __restrict__ Wf,
                                                                      const u16* __restrict__ resid, u16* __restrict__ out,
                                                                      char* __restrict__ kvout) {
  using C = GemmCfg<MODE>;
  __shared__ __attribute__((aligned(16))) char smem[C::SMEM];
  const int tid = threadIdx.x, lane = tid & 63, wave = __builtin_amdgcn_readfirstlane(tid >> 6);
  const int xcd = blockIdx.x & 7, idx = blockIdx.x >> 3;
  const int rb = xcd * 4 + idx / C::CB, cb = idx % C::CB;
  int tcol, kh, li;
  bool loader;
  if (MODE == 2) {
    loader = !((wave & 8) && !(wave & 2));
    li = wave < 8 ? wave : wave - 2;
    tcol = loader ? 4 + li : ((wave & 1) | ((wave >> 1) & 2));
    kh = 0;
  } else {
    tcol = wave % C::NT;
    kh = wave / C::NT;
    loader = wave < 8;
    li = wave;
  }
  const int gtile = cb * C::NT + tcol;
  const bool active = (MODE != 0) || (gtile < 39);
  const int row0 = rb * 64 - (MODE == 2 ? 16 : 0);
  const int mloc = lane & 15, g = lane >> 4, nq = g * 4;

  f32x4 acc[C::NACC];
#pragma unroll
  for (int t = 0; t < C::NACC; ++t) acc[t] = f32x4{0.f, 0.f, 0.f, 0.f};
  u32x2 xr[C::RT];
  f32x4 cs0, cs1;

  auto body = [&](auto ldc, auto k0c) {
    constexpr bool LD = decltype(ldc)::value;
    constexpr bool K0 = decltype(k0c)::value;
    constexpr int T0 = (MODE == 2 && !LD) ? 1 : 0;
    const int sr = li * 8 + (lane >> 3), sj = lane & 7;
    u32x4 xa[9];
    int grow = row0 + sr;
    if (MODE == 2) grow = grow < 0 ? 0 : grow;
    const u16* ap = A + (size_t)grow * DM + sj * 8;
    char* lp = smem + sr * 1152 + ((sj ^ ((sr >> 1) & 7)) * 16);
    if (LD) {
#pragma unroll
      for (int i = 0; i < 3; ++i) xa[i] = *(const u32x4*)(ap + i * 64);
    }
    __builtin_amdgcn_sched_barrier(0);
    __builtin_amdgcn_s_barrier();
    __builtin_amdgcn_sched_barrier(0);
    bf16x8 wf[C::KSTEPS];
    const char* wp = Wf + (size_t)(active ? gtile : 0) * FRAG_TILE + kh * 1024 + lane * 16;
#pragma unroll
    for (int s = 0; s < C::PF; ++s) wf[s] = *(const bf16x8*)(wp + s * C::KS * 1024);
    __builtin_amdgcn_sched_barrier(0);
    if (LD) {
#pragma unroll
      for (int i = 3; i < 9; ++i) xa[i] = *(const u32x4*)(ap + i * 64);
    }
    __builtin_amdgcn_sched_barrier(0);
    float sum = 0.f, sq = 0.f;
    auto stage = [&](int c) {
      __builtin_amdgcn_s_setprio(3);
#pragma unroll
      for (int i = 3 * c; i < 3 * c + 3; ++i) *(u32x4*)(lp + i * 128) = xa[i];
      if (MODE != 1) {
#pragma unroll
        for (int i = 3 * c; i < 3 * c + 3; ++i)
#pragma unroll
          for (int j = 0; j < 4; ++j) {
            sum = dot2_bf16(xa[i][j], 0x3f803f80u, sum);
            sq = dot2_bf16(xa[i][j], xa[i][j], sq);
          }
        if (c == 2) {
          sum = red8(sum);
          sq = red8(sq);
          const float mean = sum * (1.f / 576.f);
          float var = sq * (1.f / 576.f) - mean * mean;
          if (__any(!(mean * mean <= 16.f * var))) {
            var = 0.f;
#pragma unroll
            for (int i = 0; i < 9; ++i)
#pragma unroll
              for (int j = 0; j < 4; ++j) {
                float a = bf_lo(xa[i][j]) - mean, b = bf_hi(xa[i][j]) - mean;
                var += a * a + b * b;
              }
            var = red8(var) * (1.f / 576.f);
          }
          const float rstd = 1.0f / sqrtf(var + 1e-5f);
          if (sj == 0) *(f32x2*)(smem + C::OFF_STAT + sr * 8) = f32x2{mean, rstd};
        }
      }
      __builtin_amdgcn_s_setprio(0);
    };
    if (LD) stage(0);
    __syncthreads();
    __builtin_amdgcn_sched_barrier(0);
    const int be = (g ^ (mloc >> 1)) * 16;
    const char* pA = smem + mloc * 1152 + (kh ? be ^ 64 : be);
    const char* pB = smem + mloc * 1152 + (be ^ 64);
#pragma unroll
    for (int s = 0; s < C::KSTEPS; ++s) {
      if (s + C::PF < C::KSTEPS) wf[s + C::PF] = *(const bf16x8*)(wp + (s + C::PF) * C::KS * 1024);
      if (K0 && s + C::PF == C::KSTEPS) {
        if (MODE == 1) {
#pragma unroll
          for (int t = 0; t < C::RT; ++t) xr[t] = *(const u32x2*)(resid + (size_t)(row0 + t * 16 + mloc) * DM + gtile * 16 + nq);
        } else {
          const float* cs = (const float*)resid + (active ? gtile : 0) * 16 + nq;
          cs0 = *(const f32x4*)(cs + mloc * C::NC);
          cs1 = *(const f32x4*)(cs + (16 + (mloc & 1)) * C::NC);
        }
      }
      const char* ap2 = (C::KS == 2) ? pA + s * 128 : ((s & 1) ? pB : pA) + (s >> 1) * 128;
#pragma unroll
      for (int t = T0; t < C::RT; ++t) {
        bf16x8 af = *(const bf16x8*)(ap2 + t * 16 * 1152);
        acc[t] = __builtin_amdgcn_mfma_f32_16x16x32_bf16(wf[s], af, acc[t], 0, 0, 0);
      }
      __builtin_amdgcn_sched_barrier(0);
      if (LD && s % C::SPC == C::SPC - 2 && s / C::SPC < 2) stage(s / C::SPC + 1);
      if (s % C::SPC == C::SPC - 1 && s / C::SPC < 2) __syncthreads();
      __builtin_amdgcn_sched_barrier(0);
    }
  };
  using TT = std::integral_constant<bool, true>;
  using FF = std::integral_constant<bool, false>;
  if (MODE == 2) {
    if (loader) body(TT{}, TT{});
    else body(FF{}, TT{});
  } else {
    if (kh == 0) body(TT{}, TT{});
    else if (loader) body(TT{}, FF{});
    else body(FF{}, FF{});
  }
  if (C::KS == 2) {
    char* red = smem + C::OFF_RED + tcol * C::NACC * 1024 + lane * 16;
    if (kh == 1) {
#pragma unroll
      for (int t = 0; t < C::NACC; ++t) *(f32x4*)(red + t * 1024) = acc[t];
    }
    __syncthreads();
    if (kh == 1) return;
#pragma unroll
    for (int t = 0; t < C::NACC; ++t) acc[t] += *(const f32x4*)(red + t * 1024);
  } else {
    __syncthreads();
  }
  if (MODE != 1) {
    f32x4 colsum;
#pragma unroll
    for (int i = 0; i < 4; ++i) {
      float v = cs0[i] + (mloc < 2 ? cs1[i] : 0.f);
      v = red8(v);
      v += dpp_f<0x140>(v);
      colsum[i] = v;
    }
#pragma unroll
    for (int t = 0; t < C::RT; ++t) {
      const f32x2 st = *(const f32x2*)(smem + C::OFF_STAT + (t * 16 + mloc) * 8);
#pragma unroll
      for (int i = 0; i < 4; ++i) acc[t][i] = (acc[t][i] - st[0] * colsum[i]) * st[1];
    }
  }

  if (MODE == 1) {
    const int n = gtile * 16 + nq;
#pragma unroll
    for (int t = 0; t < C::RT; ++t) {
      const size_t off = (size_t)(row0 + t * 16 + mloc) * DM + n;
      float r0 = round_bf16(acc[t][0]), r1 = round_bf16(acc[t][1]), r2 = round_bf16(acc[t][2]), r3 = round_bf16(acc[t][3]);
      u32x2 o;
      o[0] = pack_bf16(bf_lo(xr[t][0]) + r0, bf_hi(xr[t][0]) + r1);
      o[1] = pack_bf16(bf_lo(xr[t][1]) + r2, bf_hi(xr[t][1]) + r3);
      *(u32x2*)(out + off) = o;
    }
  } else if (MODE == 0) {
    if (!active) return;
    const float qs = 0.23570226039551584f * 1.4426950408889634f;
    if (gtile < 36) {
#pragma unroll
      for (int t = 0; t < C::RT; ++t) {
        const int pos = row0 + t * 16 + mloc;
        u32x2 o;
        o[0] = pack_bf16(acc[t][0] * qs, acc[t][1] * qs);
        o[1] = pack_bf16(acc[t][2] * qs, acc[t][3] * qs);
        *(u32x2*)(out + (size_t)pos * DM + gtile * 16 + nq) = o;
      }
    } else {
#pragma unroll
      for (int t = 0; t < C::RT; ++t) {
        const int pos = row0 + t * 16 + mloc;
        const int r = pos & 31;
        char* rec = kvout + (pos >> 5) * REC;
        const unsigned w0 = pack_bf16(acc[t][0], acc[t][1]), w1 = pack_bf16(acc[t][2], acc[t][3]);
        if (gtile == 36) {
          *(u32x2*)(rec + (g >> 1) * 512 + r * 16 + (g & 1) * 8) = u32x2{w0, w1};
        } else {
          const int s = r >> 4, rr = r & 15;
          const int hh = (rr >> 2) & 1, j = ((rr >> 3) << 2) | (rr & 3);
          char* vb = rec + REC_V + s * 608 + hh * 304 + j * 2;
          const int d0 = (gtile - 37) * 16 + nq - 2;
          if (d0 < 0) *(unsigned*)(rec + REC_K1 + r * 4) = w0;
          else if (d0 < 18) {
            *(u16*)(vb + d0 * 16) = (u16)(w0 & 0xffffu);
            *(u16*)(vb + (d0 + 1) * 16) = (u16)(w0 >> 16);
          } else if (d0 == 18) *(u16*)(vb + 18 * 16) = (u16)0x3f80;
          if (d0 + 2 < 18) {
            *(u16*)(vb + (d0 + 2) * 16) = (u16)(w1 & 0xffffu);
            *(u16*)(vb + (d0 + 3) * 16) = (u16)(w1 >> 16);
          }
        }
      }
    }
  } else {
    constexpr int LDQ = 456;
#pragma unroll
    for (int t = 0; t < C::RT; ++t) {
      u32x2 o;
      o[0] = pack_bf16(acc[t][0], acc[t][1]);
      o[1] = pack_bf16(acc[t][2], acc[t][3]);
      *(u32x2*)(smem + (t * 16 + mloc) * LDQ + (tcol * 16 + nq) * 2) = o;
    }
    __syncthreads();
    if (tid < 512) {
      const int half = tid & 1, hh = ((tid >> 1) & 1) | ((tid >> 4) & 2), i = ((tid >> 2) & 7) | ((tid >> 6) << 3);
      const int pos = rb * 64 + i;
      const char* qrow = smem + (16 + i) * LDQ + hh * 36;
      unsigned q[9];
#pragma unroll
      for (int d = 0; d < 9; ++d) q[d] = *(const unsigned*)(qrow + d * 4);
      const char* kbase = qrow - 8 * half * LDQ;
      float lg[4];
      float mx = -3.0e38f;
#pragma unroll
      for (int w = 0; w < 4; ++w) {
        const char* krow = kbase - 2 * w * LDQ + 144;
        float sacc = 0.f;
#pragma unroll
        for (int d = 0; d < 9; ++d) sacc = dot2_bf16(q[d], *(const unsigned*)(krow + d * 4), sacc);
        sacc *= 0.23570226039551584f;
        lg[w] = (pos - 2 * (w + 4 * half) >= 0) ? sacc : -1e9f;
        mx = fmaxf(mx, lg[w]);
      }
      mx = fmaxf(mx, dpp_f<0xB1>(mx));
      float den = 0.f;
#pragma unroll
      for (int w = 0; w < 4; ++w) {
        lg[w] = __expf(lg[w] - mx);
        den += lg[w];
      }
      den += dpp_f<0xB1>(den);
      const float inv = 1.f / den;
      float o[18];
#pragma unroll
      for (int d = 0; d < 18; ++d) o[d] = 0.f;
#pragma unroll
      for (int w = 0; w < 4; ++w) {
        const char* vrow = kbase - 2 * w * LDQ + 288;
        const float p = lg[w] * inv;
#pragma unroll
        for (int d = 0; d < 9; ++d) {
          unsigned u = *(const unsigned*)(vrow + d * 4);
          o[2 * d] += p * bf_lo(u);
          o[2 * d + 1] += p * bf_hi(u);
        }
      }
#pragma unroll
      for (int d = 0; d < 18; ++d) o[d] += dpp_f<0xB1>(o[d]);
      unsigned* op = (unsigned*)(out + (size_t)pos * DM + (cb * 4 + hh) * 18);
      if (half == 0) {
#pragma unroll
        for (int d = 0; d < 5; ++d) op[d] = pack_bf16(o[2 * d], o[2 * d + 1]);
      } else {
#pragma unroll
        for (int d = 5; d < 9; ++d) op[d] = pack_bf16(o[2 * d], o[2 * d + 1]);
      }
    }
  }
}

#define MFMA32(a, b, c) __builtin_amdgcn_mfma_f32_32x32x16_bf16(a, b, c, 0, 0, 0)
#define EXP2(x) __builtin_amdgcn_exp2f(x)

__global__ __launch_bounds__(512) void attn_kernel(const u16* __restrict__ qin, const char* __restrict__ kvin,
                                                   u16* __restrict__ out) {
  __shared__ __attribute__((aligned(16))) char smem[66 * REC];
  const int tid = threadIdx.x, lane = tid & 63, wave = tid >> 6;
  const int r = lane & 31, h = lane >> 5;
  u32x4 st[5];
  int soff[5];
#pragma unroll
  for (int j = 0; j < 5; ++j) {
    int i = wave + 8 * j;
    soff[j] = (i > 36 ? 36 : i) * 1024 + lane * 16;
  }
#pragma unroll
  for (int j = 0; j < 5; ++j) st[j] = *(const u32x4*)(kvin + soff[j]);
  const int pos = blockIdx.x * 8 + wave;
  const u16* qp = qin + (size_t)pos * DM + r * 18;
  u32x4 q0u = *(const u32x4_a4*)(qp + h * 8);
  unsigned q1w = *(const unsigned*)(qp + 16);
  if (h) q1w = 0u;
  const bf16x8 q0 = __builtin_bit_cast(bf16x8, q0u);
  const bf16x8 q1 = __builtin_bit_cast(bf16x8, u32x4{q1w, 0u, 0u, 0u});
#pragma unroll
  for (int j = 0; j < 5; ++j) *(u32x4*)(smem + soff[j]) = st[j];
#pragma unroll
  for (int j = 0; j < 5; ++j) st[j] = *(const u32x4*)(kvin + CHUNK + soff[j]);
  __syncthreads();

  const char* k0p = smem + h * 512 + r * 16;
  const char* k1p = smem + REC_K1 + r * 4;
  const char* vp = smem + REC_V + h * 304 + (r < 18 ? r : 18) * 16;

  f32x16 zero;
#pragma unroll
  for (int i = 0; i < 16; ++i) zero[i] = 0.f;
  f32x16 acc = zero;
  f32x16 negm, sA, sB;
  {
    bf16x8 k0 = *(const bf16x8*)(k0p);
    bf16x8 k1 = __builtin_bit_cast(bf16x8, u32x4{*(const unsigned*)(k1p), 0u, 0u, 0u});
    f32x16 s = MFMA32(k0, q0, zero);
    s = MFMA32(k1, q1, s);
    float mx = s[0];
#pragma unroll
    for (int i = 1; i < 16; ++i) mx = fmaxf(mx, s[i]);
    mx = fmaxf(mx, __shfl_xor(mx, 32));
#pragma unroll
    for (int i = 0; i < 16; ++i) negm[i] = -mx;
#pragma unroll
    for (int i = 0; i < 16; ++i) sA[i] = s[i] - mx;
  }
  bf16x8 k0A = *(const bf16x8*)(k0p + REC), k0B;
  u32x4 k1A = {*(const unsigned*)(k1p + REC), 0u, 0u, 0u}, k1B = {0u, 0u, 0u, 0u};
  bf16x8 v0A = {0, 0, 0, 0, 0, 0, 0, 0}, v1A = v0A, v0B, v1B;
  u32x4 pA0 = {0u, 0u, 0u, 0u}, pA1 = pA0, pB0, pB1;

#define SB __builtin_amdgcn_sched_barrier(0);
#define TILE_BODY(OFF, SC, SN, K0C, K1C, V0C, V1C, K0N, K1N, V0N, V1N, PP0, PP1, PC0, PC1)                     \
  {                                                                                                            \
    acc = MFMA32(V0C, __builtin_bit_cast(bf16x8, PP0), acc);                                                   \
    K0N = *(const bf16x8*)(k0p + (OFF) + 2 * REC);                                                             \
    K1N[0] = *(const unsigned*)(k1p + (OFF) + 2 * REC);                                                        \
    SB                                                                                                         \
    PC0[0] = pack_bf16(EXP2(SC[0]), EXP2(SC[1]));                                                              \
    PC0[1] = pack_bf16(EXP2(SC[2]), EXP2(SC[3]));                                                              \
    SB                                                                                                         \
    acc = MFMA32(V1C, __builtin_bit_cast(bf16x8, PP1), acc);                                                   \
    V0N = *(const bf16x8*)(vp + (OFF));                                                                        \
    V1N = *(const bf16x8*)(vp + (OFF) + 608);                                                                  \
    SB                                                                                                         \
    PC0[2] = pack_bf16(EXP2(SC[4]), EXP2(SC[5]));                                                              \
    PC0[3] = pack_bf16(EXP2(SC[6]), EXP2(SC[7]));                                                              \
    SB                                                                                                         \
    SN = MFMA32(K0C, q0, negm);                                                                                \
    SB                                                                                                         \
    PC1[0] = pack_bf16(EXP2(SC[8]), EXP2(SC[9]));                                                              \
    PC1[1] = pack_bf16(EXP2(SC[10]), EXP2(SC[11]));                                                            \
    SB                                                                                                         \
    SN = MFMA32(__builtin_bit_cast(bf16x8, K1C), q1, SN);                                                        \
    SB                                                                                                         \
    PC1[2] = pack_bf16(EXP2(SC[12]), EXP2(SC[13]));                                                            \
    PC1[3] = pack_bf16(EXP2(SC[14]), EXP2(SC[15]));                                                            \
    SB                                                                                                         \
  }

  for (int t = 0; t < 64; t += 2) {
    if ((t & 15) == 8 && t < 48) {
      const int c = (t >> 4) + 1;
#pragma unroll
      for (int j = 0; j < 5; ++j) *(u32x4*)(smem + c * CHUNK + soff[j]) = st[j];
      if (c < 3) {
#pragma unroll
        for (int j = 0; j < 5; ++j) st[j] = *(const u32x4*)(kvin + (c + 1) * CHUNK + soff[j]);
      }
      asm volatile("s_waitcnt lgkmcnt(0)" ::: "memory");
      __builtin_amdgcn_s_barrier();
      asm volatile("" ::: "memory");
    }
    const int off = t * REC;
    TILE_BODY(off, sA, sB, k0A, k1A, v0A, v1A, k0B, k1B, v0B, v1B, pA0, pA1, pB0, pB1)
    TILE_BODY(off + REC, sB, sA, k0B, k1B, v0B, v1B, k0A, k1A, v0A, v1A, pB0, pB1, pA0, pA1)
  }
  acc = MFMA32(v0A, __builtin_bit_cast(bf16x8, pA0), acc);
  acc = MFMA32(v1A, __builtin_bit_cast(bf16x8, pA1), acc);

  {
    bool bad = !(acc[10] < 1.0e30f);
#pragma unroll
    for (int i = 0; i < 10; ++i) bad = bad || !(fabsf(acc[i]) < 3.0e38f);
    if (__any(bad)) {
      acc = zero;
      float m = -3.0e38f;
      for (int t = 0; t < 64; ++t) {
        bf16x8 k0 = *(const bf16x8*)(k0p + t * REC);
        bf16x8 k1 = __builtin_bit_cast(bf16x8, u32x4{*(const unsigned*)(k1p + t * REC), 0u, 0u, 0u});
        bf16x8 v0 = *(const bf16x8*)(vp + t * REC);
        bf16x8 v1 = *(const bf16x8*)(vp + t * REC + 608);
        f32x16 s = MFMA32(k0, q0, zero);
        s = MFMA32(k1, q1, s);
        float mx = s[0];
#pragma unroll
        for (int i = 1; i < 16; ++i) mx = fmaxf(mx, s[i]);
        mx = fmaxf(mx, __shfl_xor(mx, 32));
        const float mnew = fmaxf(m, mx);
        const float sc = EXP2(m - mnew);
        m = mnew;
#pragma unroll
        for (int i = 0; i < 16; ++i) acc[i] *= sc;
        u32x4 p0, p1;
#pragma unroll
        for (int i = 0; i < 4; ++i) {
          p0[i] = pack_bf16(EXP2(s[2 * i] - m), EXP2(s[2 * i + 1] - m));
          p1[i] = pack_bf16(EXP2(s[8 + 2 * i] - m), EXP2(s[8 + 2 * i + 1] - m));
        }
        acc = MFMA32(v0, __builtin_bit_cast(bf16x8, p0), acc);
        acc = MFMA32(v1, __builtin_bit_cast(bf16x8, p1), acc);
      }
    }
  }
  const float inv = 1.f / acc[10];
  u16* op = out + (size_t)pos * DM + r * 18;
  {
    unsigned a0 = pack_bf16(acc[0] * inv, acc[1] * inv), a1 = pack_bf16(acc[2] * inv, acc[3] * inv);
    unsigned b0 = pack_bf16(acc[4] * inv, acc[5] * inv), b1 = pack_bf16(acc[6] * inv, acc[7] * inv);
    unsigned* o32 = (unsigned*)(op + h * 4);
    o32[0] = a0;
    o32[1] = a1;
    o32[4] = b0;
    o32[5] = b1;
    if (!h) *(unsigned*)(op + 16) = pack_bf16(acc[8] * inv, acc[9] * inv);
  }
}

extern "C" void kernel_launch(void* const* d_in, const int* in_sizes, int n_in, void* d_out, int out_size, void* d_ws,
                              size_t ws_size, hipStream_t stream) {
  const u16* x0 = (const u16*)d_in[0];
  const u16* wq = (const u16*)d_in[1];
  const u16* wk = (const u16*)d_in[2];
  const u16* wv = (const u16*)d_in[3];
  const u16* woa = (const u16*)d_in[4];
  const u16* wqkv = (const u16*)d_in[6];
  const u16* wob = (const u16*)d_in[7];
  char* ws = (char*)d_ws;
  u16* obuf = (u16*)(ws + OFF_O);
  u16* x1 = (u16*)(ws + OFF_X1);
  u16* x2 = (u16*)(ws + OFF_X2);
  u16* x3 = (u16*)(ws + OFF_X3);
  u16* qb = (u16*)(ws + OFF_Q);
  char* kvb = ws + OFF_KV;

  prep_kernel<<<(PREP_WAVES + 3) / 4, 256, 0, stream>>>(wq, wk, wv, woa, wqkv, wob, ws);
  for (int layer = 0; layer < 2; ++layer) {
    const u16* xin = layer == 0 ? x0 : x2;
    u16* xmid = layer == 0 ? x1 : x3;
    u16* xout = layer == 0 ? x2 : (u16*)d_out;
    gemm_kernel<0><<<224, 768, 0, stream>>>(xin, ws + OFF_WA, (const u16*)(ws + OFF_CSA), qb, kvb);
    attn_kernel<<<256, 512, 0, stream>>>(qb, kvb, obuf);
    gemm_kernel<1><<<192, 768, 0, stream>>>(obuf, ws + OFF_WOA, xin, xmid, nullptr);
    gemm_kernel<2><<<256, 896, 0, stream>>>(xmid, ws + OFF_WB, (const u16*)(ws + OFF_CSB), obuf, nullptr);
    gemm_kernel<1><<<192, 768, 0, stream>>>(obuf, ws + OFF_WOB, xmid, xout, nullptr);
  }
}
```

```cpp
#include <hip/hip_runtime.h>
#include <hip/hip_bf16.h>
#include <stdint.h>
#include <type_traits>

typedef __attribute__((ext_vector_type(8))) short bf16x8;
typedef __attribute__((ext_vector_type(4))) float f32x4;
typedef __attribute__((ext_vector_type(2))) float f32x2;
typedef __attribute__((ext_vector_type(16))) float f32x16;
typedef __attribute__((ext_vector_type(4))) unsigned u32x4;
typedef __attribute__((ext_vector_type(4), aligned(4))) unsigned u32x4_a4;
typedef __attribute__((ext_vector_type(2))) unsigned u32x2;
typedef __attribute__((ext_vector_type(2))) __bf16 bf16x2_t;
typedef unsigned short u16;

#define DI __device__ __forceinline__

constexpr int SEQ = 2048;
constexpr int DM = 576;
constexpr int FRAG_TILE = 18 * 1024;

constexpr int REC = 2368;
constexpr int REC_K1 = 1024;
constexpr int REC_V = 1152;
constexpr int KV_BYTES = 64 * REC;
constexpr int CHUNK = 16 * REC;

constexpr size_t OFF_WA = 0;
constexpr size_t OFF_WOA = OFF_WA + 39 * (size_t)FRAG_TILE;
constexpr size_t OFF_WB = OFF_WOA + 36 * (size_t)FRAG_TILE;
constexpr size_t OFF_WOB = OFF_WB + 112 * (size_t)FRAG_TILE;
constexpr size_t ACT_BYTES = (size_t)SEQ * DM * 2;
constexpr size_t OFF_Q = OFF_WOB + 36 * (size_t)FRAG_TILE;
constexpr size_t OFF_KV = OFF_Q + ACT_BYTES;
constexpr size_t OFF_O = OFF_KV + KV_BYTES;
constexpr size_t OFF_X1 = OFF_O + ACT_BYTES;
constexpr size_t OFF_X2 = OFF_X1 + ACT_BYTES;
constexpr size_t OFF_X3 = OFF_X2 + ACT_BYTES;
constexpr int NCA = 39 * 16, NCB = 112 * 16;
constexpr size_t OFF_CSA = OFF_X3 + ACT_BYTES;
constexpr size_t OFF_CSB = OFF_CSA + 18 * (size_t)NCA * 4;
constexpr size_t OFF_ALT = (OFF_CSB + 18 * (size_t)NCB * 4 + 4095) & ~(size_t)4095;

DI float bf_lo(unsigned u) { return __uint_as_float(u << 16); }
DI float bf_hi(unsigned u) { return __uint_as_float(u & 0xffff0000u); }
DI unsigned pack_bf16(float a, float b) {
  f32x2 v = {a, b};
  bf16x2_t r = __builtin_convertvector(v, bf16x2_t);
  return __builtin_bit_cast(unsigned, r);
}
DI float round_bf16(float a) { return bf_lo(pack_bf16(a, 0.f)); }
template <int CTRL>
DI float dpp_f(float v) {
  return __builtin_bit_cast(float, __builtin_amdgcn_update_dpp(0, __builtin_bit_cast(int, v), CTRL, 0xf, 0xf, false));
}
DI float red8(float v) {
  v += dpp_f<0xB1>(v);
  v += dpp_f<0x4E>(v);
  v += dpp_f<0x141>(v);
  return v;
}
DI float dot2_bf16(unsigned a, unsigned b, float c) {
  return __builtin_amdgcn_fdot2_f32_bf16(__builtin_bit_cast(bf16x2_t, a), __builtin_bit_cast(bf16x2_t, b), c, false);
}

constexpr int PREP_FAST = 55 * 18;
constexpr int PREP_WAVES = PREP_FAST + 3 * 18;
__global__ __launch_bounds__(256) void prep_kernel(const u16* __restrict__ wq, const u16* __restrict__ wk,
                                                   const u16* __restrict__ wv, const u16* __restrict__ woa,
                                                   const u16* __restrict__ wqkv, const u16* __restrict__ wob,
                                                   char* __restrict__ ws) {
  const int wid = blockIdx.x * 4 + (threadIdx.x >> 6);
  const int lane = threadIdx.x & 63;
  if (wid >= PREP_WAVES) return;
  const int g = lane >> 4, c = lane & 15;
  if (wid < PREP_FAST) {
    const int grp = wid / 18, s = wid % 18;
    const u16* src;
    int ld = 576;
    bool zero = false;
    char* dst;
    float* cs = nullptr;
    if (grp < 9) {
      dst = ws + OFF_WA + (size_t)grp * 4 * FRAG_TILE;
      cs = (float*)(ws + OFF_CSA) + s * NCA + grp * 64 + c * 4;
      src = wq + grp * 64 + c * 4;
    } else if (grp < 18) {
      dst = ws + OFF_WOA + (size_t)(grp - 9) * 4 * FRAG_TILE;
      src = woa + (grp - 9) * 64 + c * 4;
    } else if (grp < 27) {
      dst = ws + OFF_WOB + (size_t)(grp - 18) * 4 * FRAG_TILE;
      src = wob + (grp - 18) * 64 + c * 4;
    } else {
      dst = ws + OFF_WB + (size_t)(grp - 27) * 4 * FRAG_TILE;
      const int np = (grp - 27) * 64 + c * 4;
      cs = (float*)(ws + OFF_CSB) + s * NCB + np;
      const int hg = np / 224, n = np % 224;
      ld = 1728;
      zero = n >= 216;
      const int nn = zero ? 0 : n;
      src = wqkv + (nn / 72) * 576 + hg * 72 + (nn % 72);
    }
    u32x2 r[8];
    const int k0 = 32 * s + 8 * g;
#pragma unroll
    for (int j = 0; j < 8; ++j) r[j] = *(const u32x2*)(src + (size_t)(k0 + j) * ld);
    if (zero) {
#pragma unroll
      for (int j = 0; j < 8; ++j) r[j] = u32x2{0u, 0u};
    }
    char* dp = dst + (size_t)(c >> 2) * FRAG_TILE + s * 1024 + (g * 16 + (c & 3) * 4) * 16;
    f32x4 csum;
#pragma unroll
    for (int q = 0; q < 4; ++q) {
      u32x4 v;
      float a = 0.f;
#pragma unroll
      for (int j = 0; j < 4; ++j) {
        v[j] = __builtin_amdgcn_perm(r[2 * j + 1][q >> 1], r[2 * j][q >> 1], (q & 1) ? 0x07060302u : 0x05040100u);
        a = dot2_bf16(v[j], 0x3f803f80u, a);
      }
      *(u32x4*)(dp + q * 16) = v;
      a += __shfl_xor(a, 16);
      a += __shfl_xor(a, 32);
      csum[q] = a;
    }
    if (cs != nullptr && g == 0) *(f32x4*)cs = csum;
  } else {
    const int w2 = wid - PREP_FAST;
    const int tile = 36 + w2 / 18, s = w2 % 18;
    const int n = tile * 16 + c;
    const u16* src = (n < 594) ? wk + (n - 576) : wv + (n < 612 ? n - 594 : 0);
    const int k0 = 32 * s + 8 * g;
    u32x4 v;
    float a = 0.f;
#pragma unroll
    for (int j = 0; j < 4; ++j) {
      unsigned lo = src[(size_t)(k0 + 2 * j) * 18];
      unsigned hi = src[(size_t)(k0 + 2 * j + 1) * 18];
      v[j] = n < 612 ? (lo | (hi << 16)) : 0u;
      a = dot2_bf16(v[j], 0x3f803f80u, a);
    }
    a += __shfl_xor(a, 16);
    a += __shfl_xor(a, 32);
    if (g == 0) ((float*)(ws + OFF_CSA))[s * NCA + n] = a;
    *(u32x4*)(ws + OFF_WA + (size_t)tile * FRAG_TILE + s * 1024 + lane * 16) = v;
  }
}

template <int MODE>
struct GemmCfg {
  static constexpr int ROWS = (MODE == 2) ? 80 : 64;
  static constexpr int RT = ROWS / 16;
  static constexpr int NT = (MODE == 2) ? 14 : 6;
  static constexpr int KS = (MODE == 2) ? 1 : 2;
  static constexpr int NW = NT * KS;
  static constexpr int KSTEPS = 18 / KS;
  static constexpr int PF = (MODE == 2) ? 5 : 4;
  static constexpr int CB = (MODE == 0) ? 7 : (MODE == 1 ? 6 : 8);
  static constexpr int NACC = RT;
  static constexpr int SPC = KSTEPS / 3;
  static constexpr int NC = (MODE == 2) ? NCB : NCA;
  static constexpr int OFF_STAT = ROWS * 1152;
  static constexpr int OFF_RED = OFF_STAT + ROWS * 8;
  static constexpr int SMEM = OFF_RED + (KS == 2 ? NT * NACC * 1024 : 0);
};

template <int MODE>
__global__ __launch_bounds__(GemmCfg<MODE>::NW * 64) void gemm_kernel(const u16* __restrict__ A, const char* __restrict__ Wf,
                                                                      const u16* __restrict__ resid, u16* __restrict__ out,
                                                                      char* __restrict__ kvout) {
  using C = GemmCfg<MODE>;
  __shared__ __attribute__((aligned(16))) char smem[C::SMEM];
  const int tid = threadIdx.x, lane = tid & 63, wave = __builtin_amdgcn_readfirstlane(tid >> 6);
  const int xcd = blockIdx.x & 7, idx = blockIdx.x >> 3;
  const int rb = xcd * 4 + idx / C::CB, cb = idx % C::CB;
  int tcol, kh, li;
  bool loader;
  if (MODE == 2) {
    loader = !((wave & 8) && !(wave & 2));
    li = wave < 8 ? wave : wave - 2;
    tcol = loader ? 4 + li : ((wave & 1) | ((wave >> 1) & 2));
    kh = 0;
  } else {
    tcol = wave % C::NT;
    kh = wave / C::NT;
    loader = wave < 8;
    li = wave;
  }
  const int gtile = cb * C::NT + tcol;
  const bool active = (MODE != 0) || (gtile < 39);
  const int row0 = rb * 64 - (MODE == 2 ? 16 : 0);
  const int mloc = lane & 15, g = lane >> 4, nq = g * 4;

  f32x4 acc[C::NACC];
#pragma unroll
  for (int t = 0; t < C::NACC; ++t) acc[t] = f32x4{0.f, 0.f, 0.f, 0.f};
  u32x2 xr[C::RT];
  f32x4 cs0, cs1;

  auto body = [&](auto ldc, auto k0c) {
    constexpr bool LD = decltype(ldc)::value;
    constexpr bool K0 = decltype(k0c)::value;
    constexpr int T0 = (MODE == 2 && !LD) ? 1 : 0;
    const int sr = li * 8 + (lane >> 3), sj = lane & 7;
    u32x4 xa[9];
    int grow = row0 + sr;
    if (MODE == 2) grow = grow < 0 ? 0 : grow;
    const u16* ap = A + (size_t)grow * DM + sj * 8;
    char* lp = smem + sr * 1152 + ((sj ^ ((sr >> 1) & 7)) * 16);
    if (LD) {
#pragma unroll
      for (int i = 0; i < 3; ++i) xa[i] = *(const u32x4*)(ap + i * 64);
    }
    __builtin_amdgcn_sched_barrier(0);
    __builtin_amdgcn_s_barrier();
    __builtin_amdgcn_sched_barrier(0);
    bf16x8 wf[C::KSTEPS];
    const char* wp = Wf + (size_t)(active ? gtile : 0) * FRAG_TILE + kh * 1024 + lane * 16;
#pragma unroll
    for (int s = 0; s < C::PF; ++s) wf[s] = *(const bf16x8*)(wp + s * C::KS * 1024);
    __builtin_amdgcn_sched_barrier(0);
    if (LD) {
#pragma unroll
      for (int i = 3; i < 9; ++i) xa[i] = *(const u32x4*)(ap + i * 64);
    }
    __builtin_amdgcn_sched_barrier(0);
    float sum = 0.f, sq = 0.f;
    auto stage = [&](int c) {
      __builtin_amdgcn_s_setprio(3);
#pragma unroll
      for (int i = 3 * c; i < 3 * c + 3; ++i) *(u32x4*)(lp + i * 128) = xa[i];
      if (MODE != 1) {
#pragma unroll
        for (int i = 3 * c; i < 3 * c + 3; ++i)
#pragma unroll
          for (int j = 0; j < 4; ++j) {
            sum = dot2_bf16(xa[i][j], 0x3f803f80u, sum);
            sq = dot2_bf16(xa[i][j], xa[i][j], sq);
          }
        if (c == 2) {
          sum = red8(sum);
          sq = red8(sq);
          const float mean = sum * (1.f / 576.f);
          float var = sq * (1.f / 576.f) - mean * mean;
          if (__any(!(mean * mean <= 16.f * var))) {
            var = 0.f;
#pragma unroll
            for (int i = 0; i < 9; ++i)
#pragma unroll
              for (int j = 0; j < 4; ++j) {
                float a = bf_lo(xa[i][j]) - mean, b = bf_hi(xa[i][j]) - mean;
                var += a * a + b * b;
              }
            var = red8(var) * (1.f / 576.f);
          }
          const float rstd = 1.0f / sqrtf(var + 1e-5f);
          if (sj == 0) *(f32x2*)(smem + C::OFF_STAT + sr * 8) = f32x2{mean, rstd};
        }
      }
      __builtin_amdgcn_s_setprio(0);
    };
    if (LD) stage(0);
    __syncthreads();
    __builtin_amdgcn_sched_barrier(0);
    const int be = (g ^ (mloc >> 1)) * 16;
    const char* pA = smem + mloc * 1152 + (kh ? be ^ 64 : be);
    const char* pB = smem + mloc * 1152 + (be ^ 64);
#pragma unroll
    for (int s = 0; s < C::KSTEPS; ++s) {
      if (s + C::PF < C::KSTEPS) wf[s + C::PF] = *(const bf16x8*)(wp + (s + C::PF) * C::KS * 1024);
      if (K0 && s + C::PF == (MODE == 1 ? C::KSTEPS : C::KSTEPS - 3)) {
        if (MODE == 1) {
#pragma unroll
          for (int t = 0; t < C::RT; ++t) xr[t] = *(const u32x2*)(resid + (size_t)(row0 + t * 16 + mloc) * DM + gtile * 16 + nq);
        } else {
          const float* cs = (const float*)resid + (active ? gtile : 0) * 16 + nq;
          cs0 = *(const f32x4*)(cs + mloc * C::NC);
          cs1 = *(const f32x4*)(cs + (16 + (mloc & 1)) * C::NC);
        }
      }
      const char* ap2 = (C::KS == 2) ? pA + s * 128 : ((s & 1) ? pB : pA) + (s >> 1) * 128;
#pragma unroll
      for (int t = T0; t < C::RT; ++t) {
        bf16x8 af = *(const bf16x8*)(ap2 + t * 16 * 1152);
        acc[t] = __builtin_amdgcn_mfma_f32_16x16x32_bf16(wf[s], af, acc[t], 0, 0, 0);
      }
      __builtin_amdgcn_sched_barrier(0);
      if (LD && s % C::SPC == C::SPC - 2 && s / C::SPC < 2) stage(s / C::SPC + 1);
      if (s % C::SPC == C::SPC - 1 && s / C::SPC < 2) __syncthreads();
      __builtin_amdgcn_sched_barrier(0);
    }
  };
  using TT = std::integral_constant<bool, true>;
  using FF = std::integral_constant<bool, false>;
  if (MODE == 2) {
    if (loader) body(TT{}, TT{});
    else body(FF{}, TT{});
  } else {
    if (kh == 0) body(TT{}, TT{});
    else if (loader) body(TT{}, FF{});
    else body(FF{}, FF{});
  }
  if (C::KS == 2) {
    char* red = smem + C::OFF_RED + tcol * C::NACC * 1024 + lane * 16;
    if (kh == 1) {
#pragma unroll
      for (int t = 0; t < C::NACC; ++t) *(f32x4*)(red + t * 1024) = acc[t];
    }
    __syncthreads();
    if (kh == 1) return;
#pragma unroll
    for (int t = 0; t < C::NACC; ++t) acc[t] += *(const f32x4*)(red + t * 1024);
  } else {
    __syncthreads();
  }
  if (MODE != 1) {
    f32x4 colsum;
#pragma unroll
    for (int i = 0; i < 4; ++i) {
      float v = cs0[i] + (mloc < 2 ? cs1[i] : 0.f);
      v = red8(v);
      v += dpp_f<0x140>(v);
      colsum[i] = v;
    }
#pragma unroll
    for (int t = 0; t < C::RT; ++t) {
      const f32x2 st = *(const f32x2*)(smem + C::OFF_STAT + (t * 16 + mloc) * 8);
#pragma unroll
      for (int i = 0; i < 4; ++i) acc[t][i] = (acc[t][i] - st[0] * colsum[i]) * st[1];
    }
  }

  if (MODE == 1) {
    const int n = gtile * 16 + nq;
#pragma unroll
    for (int t = 0; t < C::RT; ++t) {
      const size_t off = (size_t)(row0 + t * 16 + mloc) * DM + n;
      float r0 = round_bf16(acc[t][0]), r1 = round_bf16(acc[t][1]), r2 = round_bf16(acc[t][2]), r3 = round_bf16(acc[t][3]);
      u32x2 o;
      o[0] = pack_bf16(bf_lo(xr[t][0]) + r0, bf_hi(xr[t][0]) + r1);
      o[1] = pack_bf16(bf_lo(xr[t][1]) + r2, bf_hi(xr[t][1]) + r3);
      *(u32x2*)(out + off) = o;
    }
  } else if (MODE == 0) {
    if (!active) return;
    const float qs = 0.23570226039551584f * 1.4426950408889634f;
    if (gtile < 36) {
#pragma unroll
      for (int t = 0; t < C::RT; ++t) {
        const int pos = row0 + t * 16 + mloc;
        u32x2 o;
        o[0] = pack_bf16(acc[t][0] * qs, acc[t][1] * qs);
        o[1] = pack_bf16(acc[t][2] * qs, acc[t][3] * qs);
        *(u32x2*)(out + (size_t)pos * DM + gtile * 16 + nq) = o;
      }
    } else {
#pragma unroll
      for (int t = 0; t < C::RT; ++t) {
        const int pos = row0 + t * 16 + mloc;
        const int r = pos & 31;
        char* rec = kvout + (pos >> 5) * REC;
        const unsigned w0 = pack_bf16(acc[t][0], acc[t][1]), w1 = pack_bf16(acc[t][2], acc[t][3]);
        if (gtile == 36) {
          *(u32x2*)(rec + (g >> 1) * 512 + r * 16 + (g & 1) * 8) = u32x2{w0, w1};
        } else {
          const int s = r >> 4, rr = r & 15;
          const int hh = (rr >> 2) & 1, j = ((rr >> 3) << 2) | (rr & 3);
          char* vb = rec + REC_V + s * 608 + hh * 304 + j * 2;
          const int d0 = (gtile - 37) * 16 + nq - 2;
          if (d0 < 0) *(unsigned*)(rec + REC_K1 + r * 4) = w0;
          else if (d0 < 18) {
            *(u16*)(vb + d0 * 16) = (u16)(w0 & 0xffffu);
            *(u16*)(vb + (d0 + 1) * 16) = (u16)(w0 >> 16);
          } else if (d0 == 18) *(u16*)(vb + 18 * 16) = (u16)0x3f80;
          if (d0 + 2 < 18) {
            *(u16*)(vb + (d0 + 2) * 16) = (u16)(w1 & 0xffffu);
            *(u16*)(vb + (d0 + 3) * 16) = (u16)(w1 >> 16);
          }
        }
      }
    }
  } else {
    constexpr int LDQ = 456;
#pragma unroll
    for (int t = 0; t < C::RT; ++t) {
      u32x2 o;
      o[0] = pack_bf16(acc[t][0], acc[t][1]);
      o[1] = pack_bf16(acc[t][2], acc[t][3]);
      *(u32x2*)(smem + (t * 16 + mloc) * LDQ + (tcol * 16 + nq) * 2) = o;
    }
    __syncthreads();
    if (tid < 512) {
      const int half = tid & 1, hh = ((tid >> 1) & 1) | ((tid >> 4) & 2), i = ((tid >> 2) & 7) | ((tid >> 6) << 3);
      const int pos = rb * 64 + i;
      const char* qrow = smem + (16 + i) * LDQ + hh * 36;
      unsigned q[9];
#pragma unroll
      for (int d = 0; d < 9; ++d) q[d] = *(const unsigned*)(qrow + d * 4);
      const char* kbase = qrow - 8 * half * LDQ;
      float lg[4];
      float mx = -3.0e38f;
#pragma unroll
      for (int w = 0; w < 4; ++w) {
        const char* krow = kbase - 2 * w * LDQ + 144;
        float sacc = 0.f;
#pragma unroll
        for (int d = 0; d < 9; ++d) sacc = dot2_bf16(q[d], *(const unsigned*)(krow + d * 4), sacc);
        sacc *= 0.23570226039551584f;
        lg[w] = (pos - 2 * (w + 4 * half) >= 0) ? sacc : -1e9f;
        mx = fmaxf(mx, lg[w]);
      }
      mx = fmaxf(mx, dpp_f<0xB1>(mx));
      float den = 0.f;
#pragma unroll
      for (int w = 0; w < 4; ++w) {
        lg[w] = __expf(lg[w] - mx);
        den += lg[w];
      }
      den += dpp_f<0xB1>(den);
      const float inv = 1.f / den;
      float o[18];
#pragma unroll
      for (int d = 0; d < 18; ++d) o[d] = 0.f;
#pragma unroll
      for (int w = 0; w < 4; ++w) {
        const char* vrow = kbase - 2 * w * LDQ + 288;
        const float p = lg[w] * inv;
#pragma unroll
        for (int d = 0; d < 9; ++d) {
          unsigned u = *(const unsigned*)(vrow + d * 4);
          o[2 * d] += p * bf_lo(u);
          o[2 * d + 1] += p * bf_hi(u);
        }
      }
#pragma unroll
      for (int d = 0; d < 18; ++d) o[d] += dpp_f<0xB1>(o[d]);
      unsigned* op = (unsigned*)(out + (size_t)pos * DM + (cb * 4 + hh) * 18);
      if (half == 0) {
#pragma unroll
        for (int d = 0; d < 5; ++d) op[d] = pack_bf16(o[2 * d], o[2 * d + 1]);
      } else {
#pragma unroll
        for (int d = 5; d < 9; ++d) op[d] = pack_bf16(o[2 * d], o[2 * d + 1]);
      }
    }
  }
}

#define MFMA32(a, b, c) __builtin_amdgcn_mfma_f32_32x32x16_bf16(a, b, c, 0, 0, 0)
#define EXP2(x) __builtin_amdgcn_exp2f(x)

__global__ __launch_bounds__(512) void attn_kernel(const u16* __restrict__ qin, const char* __restrict__ kvin,
                                                   u16* __restrict__ out) {
  __shared__ __attribute__((aligned(16))) char smem[66 * REC];
  const int tid = threadIdx.x, lane = tid & 63, wave = tid >> 6;
  const int r = lane & 31, h = lane >> 5;
  u32x4 st[5];
  int soff[5];
#pragma unroll
  for (int j = 0; j < 5; ++j) {
    int i = wave + 8 * j;
    soff[j] = (i > 36 ? 36 : i) * 1024 + lane * 16;
  }
#pragma unroll
  for (int j = 0; j < 5; ++j) st[j] = *(const u32x4*)(kvin + soff[j]);
  const int pos = blockIdx.x * 8 + wave;
  const u16* qp = qin + (size_t)pos * DM + r * 18;
  u32x4 q0u = *(const u32x4_a4*)(qp + h * 8);
  unsigned q1w = *(const unsigned*)(qp + 16);
  if (h) q1w = 0u;
  const bf16x8 q0 = __builtin_bit_cast(bf16x8, q0u);
  const bf16x8 q1 = __builtin_bit_cast(bf16x8, u32x4{q1w, 0u, 0u, 0u});
#pragma unroll
  for (int j = 0; j < 5; ++j) *(u32x4*)(smem + soff[j]) = st[j];
#pragma unroll
  for (int j = 0; j < 5; ++j) st[j] = *(const u32x4*)(kvin + CHUNK + soff[j]);
  __syncthreads();

  const char* k0p = smem + h * 512 + r * 16;
  const char* k1p = smem + REC_K1 + r * 4;
  const char* vp = smem + REC_V + h * 304 + (r < 18 ? r : 18) * 16;

  f32x16 zero;
#pragma unroll
  for (int i = 0; i < 16; ++i) zero[i] = 0.f;
  f32x16 acc = zero;
  f32x16 negm, sA, sB;
  {
    bf16x8 k0 = *(const bf16x8*)(k0p);
    bf16x8 k1 = __builtin_bit_cast(bf16x8, u32x4{*(const unsigned*)(k1p), 0u, 0u, 0u});
    f32x16 s = MFMA32(k0, q0, zero);
    s = MFMA32(k1, q1, s);
    float mx = s[0];
#pragma unroll
    for (int i = 1; i < 16; ++i) mx = fmaxf(mx, s[i]);
    mx = fmaxf(mx, __shfl_xor(mx, 32));
#pragma unroll
    for (int i = 0; i < 16; ++i) negm[i] = -mx;
#pragma unroll
    for (int i = 0; i < 16; ++i) sA[i] = s[i] - mx;
  }
  bf16x8 k0A = *(const bf16x8*)(k0p + REC), k0B;
  u32x4 k1A = {*(const unsigned*)(k1p + REC), 0u, 0u, 0u}, k1B = {0u, 0u, 0u, 0u};
  bf16x8 v0A = {0, 0, 0, 0, 0, 0, 0, 0}, v1A = v0A, v0B, v1B;
  u32x4 pA0 = {0u, 0u, 0u, 0u}, pA1 = pA0, pB0, pB1;

#define SB __builtin_amdgcn_sched_barrier(0);
#define TILE_BODY(OFF, SC, SN, K0C, K1C, V0C, V1C, K0N, K1N, V0N, V1N, PP0, PP1, PC0, PC1)                     \
  {                                                                                                            \
    acc = MFMA32(V0C, __builtin_bit_cast(bf16x8, PP0), acc);                                                   \
    K0N = *(const bf16x8*)(k0p + (OFF) + 2 * REC);                                                             \
    K1N[0] = *(const unsigned*)(k1p + (OFF) + 2 * REC);                                                        \
    SB                                                                                                         \
    PC0[0] = pack_bf16(EXP2(SC[0]), EXP2(SC[1]));                                                              \
    PC0[1] = pack_bf16(EXP2(SC[2]), EXP2(SC[3]));                                                              \
    SB                                                                                                         \
    acc = MFMA32(V1C, __builtin_bit_cast(bf16x8, PP1), acc);                                                   \
    V0N = *(const bf16x8*)(vp + (OFF));                                                                        \
    V1N = *(const bf16x8*)(vp + (OFF) + 608);                                                                  \
    SB                                                                                                         \
    PC0[2] = pack_bf16(EXP2(SC[4]), EXP2(SC[5]));                                                              \
    PC0[3] = pack_bf16(EXP2(SC[6]), EXP2(SC[7]));                                                              \
    SB                                                                                                         \
    SN = MFMA32(K0C, q0, negm);                                                                                \
    SB                                                                                                         \
    PC1[0] = pack_bf16(EXP2(SC[8]), EXP2(SC[9]));                                                              \
    PC1[1] = pack_bf16(EXP2(SC[10]), EXP2(SC[11]));                                                            \
    SB                                                                                                         \
    SN = MFMA32(__builtin_bit_cast(bf16x8, K1C), q1, SN);                                                        \
    SB                                                                                                         \
    PC1[2] = pack_bf16(EXP2(SC[12]), EXP2(SC[13]));                                                            \
    PC1[3] = pack_bf16(EXP2(SC[14]), EXP2(SC[15]));                                                            \
    SB                                                                                                         \
  }

  for (int t = 0; t < 64; t += 2) {
    if ((t & 15) == 8 && t < 48) {
      const int c = (t >> 4) + 1;
#pragma unroll
      for (int j = 0; j < 5; ++j) *(u32x4*)(smem + c * CHUNK + soff[j]) = st[j];
      if (c < 3) {
#pragma unroll
        for (int j = 0; j < 5; ++j) st[j] = *(const u32x4*)(kvin + (c + 1) * CHUNK + soff[j]);
      }
      asm volatile("s_waitcnt lgkmcnt(0)" ::: "memory");
      __builtin_amdgcn_s_barrier();
      asm volatile("" ::: "memory");
    }
    const int off = t * REC;
    TILE_BODY(off, sA, sB, k0A, k1A, v0A, v1A, k0B, k1B, v0B, v1B, pA0, pA1, pB0, pB1)
    TILE_BODY(off + REC, sB, sA, k0B, k1B, v0B, v1B, k0A, k1A, v0A, v1A, pB0, pB1, pA0, pA1)
  }
  acc = MFMA32(v0A, __builtin_bit_cast(bf16x8, pA0), acc);
  acc = MFMA32(v1A, __builtin_bit_cast(bf16x8, pA1), acc);

  {
    bool bad = !(acc[10] < 1.0e30f);
#pragma unroll
    for (int i = 0; i < 10; ++i) bad = bad || !(fabsf(acc[i]) < 3.0e38f);
    if (__any(bad)) {
      acc = zero;
      float m = -3.0e38f;
      for (int t = 0; t < 64; ++t) {
        bf16x8 k0 = *(const bf16x8*)(k0p + t * REC);
        bf16x8 k1 = __builtin_bit_cast(bf16x8, u32x4{*(const unsigned*)(k1p + t * REC), 0u, 0u, 0u});
        bf16x8 v0 = *(const bf16x8*)(vp + t * REC);
        bf16x8 v1 = *(const bf16x8*)(vp + t * REC + 608);
        f32x16 s = MFMA32(k0, q0, zero);
        s = MFMA32(k1, q1, s);
        float mx = s[0];
#pragma unroll
        for (int i = 1; i < 16; ++i) mx = fmaxf(mx, s[i]);
        mx = fmaxf(mx, __shfl_xor(mx, 32));
        const float mnew = fmaxf(m, mx);
        const float sc = EXP2(m - mnew);
        m = mnew;
#pragma unroll
        for (int i = 0; i < 16; ++i) acc[i] *= sc;
        u32x4 p0, p1;
#pragma unroll
        for (int i = 0; i < 4; ++i) {
          p0[i] = pack_bf16(EXP2(s[2 * i] - m), EXP2(s[2 * i + 1] - m));
          p1[i] = pack_bf16(EXP2(s[8 + 2 * i] - m), EXP2(s[8 + 2 * i + 1] - m));
        }
        acc = MFMA32(v0, __builtin_bit_cast(bf16x8, p0), acc);
        acc = MFMA32(v1, __builtin_bit_cast(bf16x8, p1), acc);
      }
    }
  }
  const float inv = 1.f / acc[10];
  u16* op = out + (size_t)pos * DM + r * 18;
  {
    unsigned a0 = pack_bf16(acc[0] * inv, acc[1] * inv), a1 = pack_bf16(acc[2] * inv, acc[3] * inv);
    unsigned b0 = pack_bf16(acc[4] * inv, acc[5] * inv), b1 = pack_bf16(acc[6] * inv, acc[7] * inv);
    unsigned* o32 = (unsigned*)(op + h * 4);
    o32[0] = a0;
    o32[1] = a1;
    o32[4] = b0;
    o32[5] = b1;
    if (!h) *(unsigned*)(op + 16) = pack_bf16(acc[8] * inv, acc[9] * inv);
  }
}

extern "C" void kernel_launch(void* const* d_in, const int* in_sizes, int n_in, void* d_out, int out_size, void* d_ws,
                              size_t ws_size, hipStream_t stream) {
  const u16* x0 = (const u16*)d_in[0];
  const u16* wq = (const u16*)d_in[1];
  const u16* wk = (const u16*)d_in[2];
  const u16* wv = (const u16*)d_in[3];
  const u16* woa = (const u16*)d_in[4];
  const u16* wqkv = (const u16*)d_in[6];
  const u16* wob = (const u16*)d_in[7];
  char* ws = (char*)d_ws;
  u16* x1 = (u16*)(ws + OFF_X1);
  u16* x2 = (u16*)(ws + OFF_X2);
  u16* x3 = (u16*)(ws + OFF_X3);

  prep_kernel<<<(PREP_WAVES + 3) / 4, 256, 0, stream>>>(wq, wk, wv, woa, wqkv, wob, ws);
  for (int layer = 0; layer < 2; ++layer) {
    const u16* xin = layer == 0 ? x0 : x2;
    u16* xmid = layer == 0 ? x1 : x3;
    u16* xout = layer == 0 ? x2 : (u16*)d_out;
    u16* qb = (u16*)(ws + (layer == 0 ? OFF_Q : OFF_ALT));
    char* kvb = ws + (layer == 0 ? OFF_KV : OFF_ALT + ACT_BYTES);
    u16* obuf = (u16*)(ws + (layer == 0 ? OFF_O : OFF_ALT + 2 * ACT_BYTES));
    u16* obuf2 = (u16*)(ws + OFF_ALT + (3 + layer) * ACT_BYTES);
    gemm_kernel<0><<<224, 768, 0, stream>>>(xin, ws + OFF_WA, (const u16*)(ws + OFF_CSA), qb, kvb);
    attn_kernel<<<256, 512, 0, stream>>>(qb, kvb, obuf);
    gemm_kernel<1><<<192, 768, 0, stream>>>(obuf, ws + OFF_WOA, xin, xmid, nullptr);
    gemm_kernel<2><<<256, 896, 0, stream>>>(xmid, ws + OFF_WB, (const u16*)(ws + OFF_CSB), obuf2, nullptr);
    gemm_kernel<1><<<192, 768, 0, stream>>>(obuf2, ws + OFF_WOB, xmid, xout, nullptr);
  }
}
```

```cpp
#include <hip/hip_runtime.h>
#include <hip/hip_bf16.h>
#include <stdint.h>
#include <type_traits>

typedef __attribute__((ext_vector_type(8))) short bf16x8;
typedef __attribute__((ext_vector_type(4))) float f32x4;
typedef __attribute__((ext_vector_type(2))) float f32x2;
typedef __attribute__((ext_vector_type(16))) float f32x16;
typedef __attribute__((ext_vector_type(4))) unsigned u32x4;
typedef __attribute__((ext_vector_type(4), aligned(4))) unsigned u32x4_a4;
typedef __attribute__((ext_vector_type(2))) unsigned u32x2;
typedef __attribute__((ext_vector_type(2))) __bf16 bf16x2_t;
typedef unsigned short u16;

#define DI __device__ __forceinline__

constexpr int SEQ = 2048;
constexpr int DM = 576;
constexpr int FRAG_TILE = 18 * 1024;

constexpr int REC = 2368;
constexpr int REC_K1 = 1024;
constexpr int REC_V = 1152;
constexpr int KV_BYTES = 64 * REC;
constexpr int CHUNK = 16 * REC;

constexpr size_t OFF_WA = 0;
constexpr size_t OFF_WOA = OFF_WA + 39 * (size_t)FRAG_TILE;
constexpr size_t OFF_WB = OFF_WOA + 36 * (size_t)FRAG_TILE;
constexpr size_t OFF_WOB = OFF_WB + 112 * (size_t)FRAG_TILE;
constexpr size_t ACT_BYTES = (size_t)SEQ * DM * 2;
constexpr size_t OFF_Q = OFF_WOB + 36 * (size_t)FRAG_TILE;
constexpr size_t OFF_KV = OFF_Q + ACT_BYTES;
constexpr size_t OFF_O = OFF_KV + KV_BYTES;
constexpr size_t OFF_X1 = OFF_O + ACT_BYTES;
constexpr size_t OFF_X2 = OFF_X1 + ACT_BYTES;
constexpr size_t OFF_X3 = OFF_X2 + ACT_BYTES;
constexpr int NCA = 39 * 16, NCB = 112 * 16;
constexpr size_t OFF_CSA = OFF_X3 + ACT_BYTES;
constexpr size_t OFF_CSB = OFF_CSA + 18 * (size_t)NCA * 4;
constexpr size_t OFF_ALT = (OFF_CSB + 18 * (size_t)NCB * 4 + 4095) & ~(size_t)4095;

DI float bf_lo(unsigned u) { return __uint_as_float(u << 16); }
DI float bf_hi(unsigned u) { return __uint_as_float(u & 0xffff0000u); }
DI unsigned pack_bf16(float a, float b) {
  f32x2 v = {a, b};
  bf16x2_t r = __builtin_convertvector(v, bf16x2_t);
  return __builtin_bit_cast(unsigned, r);
}
DI float round_bf16(float a) { return bf_lo(pack_bf16(a, 0.f)); }
template <int CTRL>
DI float dpp_f(float v) {
  return __builtin_bit_cast(float, __builtin_amdgcn_update_dpp(0, __builtin_bit_cast(int, v), CTRL, 0xf, 0xf, false));
}
DI float red8(float v) {
  v += dpp_f<0xB1>(v);
  v += dpp_f<0x4E>(v);
  v += dpp_f<0x141>(v);
  return v;
}
DI float dot2_bf16(unsigned a, unsigned b, float c) {
  return __builtin_amdgcn_fdot2_f32_bf16(__builtin_bit_cast(bf16x2_t, a), __builtin_bit_cast(bf16x2_t, b), c, false);
}

constexpr int PREP_FAST = 55 * 18;
constexpr int PREP_WAVES = PREP_FAST + 3 * 18;
__global__ __launch_bounds__(256) void prep_kernel(const u16* __restrict__ wq, const u16* __restrict__ wk,
                                                   const u16* __restrict__ wv, const u16* __restrict__ woa,
                                                   const u16* __restrict__ wqkv, const u16* __restrict__ wob,
                                                   char* __restrict__ ws) {
  const int wid = blockIdx.x * 4 + (threadIdx.x >> 6);
  const int lane = threadIdx.x & 63;
  if (wid >= PREP_WAVES) return;
  const int g = lane >> 4, c = lane & 15;
  if (wid < PREP_FAST) {
    const int grp = wid / 18, s = wid % 18;
    const u16* src;
    int ld = 576;
    bool zero = false;
    char* dst;
    float* cs = nullptr;
    if (grp < 9) {
      dst = ws + OFF_WA + (size_t)grp * 4 * FRAG_TILE;
      cs = (float*)(ws + OFF_CSA) + s * NCA + grp * 64 + c * 4;
      src = wq + grp * 64 + c * 4;
    } else if (grp < 18) {
      dst = ws + OFF_WOA + (size_t)(grp - 9) * 4 * FRAG_TILE;
      src = woa + (grp - 9) * 64 + c * 4;
    } else if (grp < 27) {
      dst = ws + OFF_WOB + (size_t)(grp - 18) * 4 * FRAG_TILE;
      src = wob + (grp - 18) * 64 + c * 4;
    } else {
      dst = ws + OFF_WB + (size_t)(grp - 27) * 4 * FRAG_TILE;
      const int np = (grp - 27) * 64 + c * 4;
      cs = (float*)(ws + OFF_CSB) + s * NCB + np;
      const int hg = np / 224, n = np % 224;
      ld = 1728;
      zero = n >= 216;
      const int nn = zero ? 0 : n;
      src = wqkv + (nn / 72) * 576 + hg * 72 + (nn % 72);
    }
    u32x2 r[8];
    const int k0 = 32 * s + 8 * g;
#pragma unroll
    for (int j = 0; j < 8; ++j) r[j] = *(const u32x2*)(src + (size_t)(k0 + j) * ld);
    if (zero) {
#pragma unroll
      for (int j = 0; j < 8; ++j) r[j] = u32x2{0u, 0u};
    }
    char* dp = dst + (size_t)(c >> 2) * FRAG_TILE + s * 1024 + (g * 16 + (c & 3) * 4) * 16;
    f32x4 csum;
#pragma unroll
    for (int q = 0; q < 4; ++q) {
      u32x4 v;
      float a = 0.f;
#pragma unroll
      for (int j = 0; j < 4; ++j) {
        v[j] = __builtin_amdgcn_perm(r[2 * j + 1][q >> 1], r[2 * j][q >> 1], (q & 1) ? 0x07060302u : 0x05040100u);
        a = dot2_bf16(v[j], 0x3f803f80u, a);
      }
      *(u32x4*)(dp + q * 16) = v;
      a += __shfl_xor(a, 16);
      a += __shfl_xor(a, 32);
      csum[q] = a;
    }
    if (cs != nullptr && g == 0) *(f32x4*)cs = csum;
  } else {
    const int w2 = wid - PREP_FAST;
    const int tile = 36 + w2 / 18, s = w2 % 18;
    const int n = tile * 16 + c;
    const u16* src = (n < 594) ? wk + (n - 576) : wv + (n < 612 ? n - 594 : 0);
    const int k0 = 32 * s + 8 * g;
    u32x4 v;
    float a = 0.f;
#pragma unroll
    for (int j = 0; j < 4; ++j) {
      unsigned lo = src[(size_t)(k0 + 2 * j) * 18];
      unsigned hi = src[(size_t)(k0 + 2 * j + 1) * 18];
      v[j] = n < 612 ? (lo | (hi << 16)) : 0u;
      a = dot2_bf16(v[j], 0x3f803f80u, a);
    }
    a += __shfl_xor(a, 16);
    a += __shfl_xor(a, 32);
    if (g == 0) ((float*)(ws + OFF_CSA))[s * NCA + n] = a;
    *(u32x4*)(ws + OFF_WA + (size_t)tile * FRAG_TILE + s * 1024 + lane * 16) = v;
  }
}

template <int MODE>
struct GemmCfg {
  static constexpr int ROWS = (MODE == 2) ? 80 : 64;
  static constexpr int RT = ROWS / 16;
  static constexpr int NT = (MODE == 2) ? 14 : 6;
  static constexpr int KS = (MODE == 2) ? 1 : 2;
  static constexpr int NW = NT * KS;
  static constexpr int KSTEPS = 18 / KS;
  static constexpr int PF = (MODE == 2) ? 5 : 4;
  static constexpr int CB = (MODE == 0) ? 7 : (MODE == 1 ? 6 : 8);
  static constexpr int NACC = RT;
  static constexpr int SPC = KSTEPS / 3;
  static constexpr int NC = (MODE == 2) ? NCB : NCA;
  static constexpr int OFF_STAT = ROWS * 1152;
  static constexpr int OFF_RED = OFF_STAT + ROWS * 8;
  static constexpr int SMEM = OFF_RED + (KS == 2 ? NT * NACC * 1024 : 0);
};

template <int MODE>
__global__ __launch_bounds__(GemmCfg<MODE>::NW * 64) void gemm_kernel(const u16* __restrict__ A, const char* __restrict__ Wf,
                                                                      const u16* __restrict__ resid, u16* __restrict__ out,
                                                                      char* __restrict__ kvout) {
  using C = GemmCfg<MODE>;
  __shared__ __attribute__((aligned(16))) char smem[C::SMEM];
  const int tid = threadIdx.x, lane = tid & 63, wave = __builtin_amdgcn_readfirstlane(tid >> 6);
  const int xcd = blockIdx.x & 7, idx = blockIdx.x >> 3;
  const int rb = xcd * 4 + idx / C::CB, cb = idx % C::CB;
  int tcol, kh, li;
  bool loader;
  if (MODE == 2) {
    loader = !((wave & 8) && !(wave & 2));
    li = wave < 8 ? wave : wave - 2;
    tcol = loader ? 4 + li : ((wave & 1) | ((wave >> 1) & 2));
    kh = 0;
  } else {
    tcol = wave % C::NT;
    kh = wave / C::NT;
    loader = wave < 8;
    li = wave;
  }
  const int gtile = cb * C::NT + tcol;
  const bool active = (MODE != 0) || (gtile < 39);
  const int row0 = rb * 64 - (MODE == 2 ? 16 : 0);
  const int mloc = lane & 15, g = lane >> 4, nq = g * 4;

  f32x4 acc[C::NACC];
#pragma unroll
  for (int t = 0; t < C::NACC; ++t) acc[t] = f32x4{0.f, 0.f, 0.f, 0.f};
  u32x2 xr[C::RT];
  f32x4 cs0, cs1;

  auto body = [&](auto ldc, auto k0c) {
    constexpr bool LD = decltype(ldc)::value;
    constexpr bool K0 = decltype(k0c)::value;
    constexpr int T0 = (MODE == 2 && !LD) ? 1 : 0;
    const int sr = li * 8 + (lane >> 3), sj = lane & 7;
    u32x4 xa[9];
    int grow = row0 + sr;
    if (MODE == 2) grow = grow < 0 ? 0 : grow;
    const u16* ap = A + (size_t)grow * DM + sj * 8;
    char* lp = smem + sr * 1152 + ((sj ^ ((sr >> 1) & 7)) * 16);
    if (LD) {
#pragma unroll
      for (int i = 0; i < 3; ++i) xa[i] = *(const u32x4*)(ap + i * 64);
    }
    __builtin_amdgcn_sched_barrier(0);
    __builtin_amdgcn_s_barrier();
    __builtin_amdgcn_sched_barrier(0);
    bf16x8 wf[C::KSTEPS];
    const char* wp = Wf + (size_t)(active ? gtile : 0) * FRAG_TILE + kh * 1024 + lane * 16;
#pragma unroll
    for (int s = 0; s < C::PF; ++s) wf[s] = *(const bf16x8*)(wp + s * C::KS * 1024);
    __builtin_amdgcn_sched_barrier(0);
    if (LD) {
#pragma unroll
      for (int i = 3; i < 9; ++i) xa[i] = *(const u32x4*)(ap + i * 64);
    }
    __builtin_amdgcn_sched_barrier(0);
    float sum = 0.f, sq = 0.f;
    auto stage = [&](int c) {
      __builtin_amdgcn_s_setprio(3);
#pragma unroll
      for (int i = 3 * c; i < 3 * c + 3; ++i) *(u32x4*)(lp + i * 128) = xa[i];
      if (MODE != 1) {
#pragma unroll
        for (int i = 3 * c; i < 3 * c + 3; ++i)
#pragma unroll
          for (int j = 0; j < 4; ++j) {
            sum = dot2_bf16(xa[i][j], 0x3f803f80u, sum);
            sq = dot2_bf16(xa[i][j], xa[i][j], sq);
          }
        if (c == 2) {
          sum = red8(sum);
          sq = red8(sq);
          const float mean = sum * (1.f / 576.f);
          float var = sq * (1.f / 576.f) - mean * mean;
          if (__any(!(mean * mean <= 16.f * var))) {
            var = 0.f;
#pragma unroll
            for (int i = 0; i < 9; ++i)
#pragma unroll
              for (int j = 0; j < 4; ++j) {
                float a = bf_lo(xa[i][j]) - mean, b = bf_hi(xa[i][j]) - mean;
                var += a * a + b * b;
              }
            var = red8(var) * (1.f / 576.f);
          }
          const float rstd = 1.0f / sqrtf(var + 1e-5f);
          if (sj == 0) *(f32x2*)(smem + C::OFF_STAT + sr * 8) = f32x2{mean, rstd};
        }
      }
      __builtin_amdgcn_s_setprio(0);
    };
    if (LD) stage(0);
    __syncthreads();
    __builtin_amdgcn_sched_barrier(0);
    const int be = (g ^ (mloc >> 1)) * 16;
    const char* pA = smem + mloc * 1152 + (kh ? be ^ 64 : be);
    const char* pB = smem + mloc * 1152 + (be ^ 64);
#pragma unroll
    for (int s = 0; s < C::KSTEPS; ++s) {
      if (s + C::PF < C::KSTEPS) wf[s + C::PF] = *(const bf16x8*)(wp + (s + C::PF) * C::KS * 1024);
      if (K0 && s + C::PF == (MODE == 1 ? C::KSTEPS : C::KSTEPS - 3)) {
        if (MODE == 1) {
#pragma unroll
          for (int t = 0; t < C::RT; ++t) xr[t] = *(const u32x2*)(resid + (size_t)(row0 + t * 16 + mloc) * DM + gtile * 16 + nq);
        } else {
          const float* cs = (const float*)resid + (active ? gtile : 0) * 16 + nq;
          cs0 = *(const f32x4*)(cs + mloc * C::NC);
          cs1 = *(const f32x4*)(cs + (16 + (mloc & 1)) * C::NC);
        }
      }
      const char* ap2 = (C::KS == 2) ? pA + s * 128 : ((s & 1) ? pB : pA) + (s >> 1) * 128;
#pragma unroll
      for (int t = T0; t < C::RT; ++t) {
        bf16x8 af = *(const bf16x8*)(ap2 + t * 16 * 1152);
        acc[t] = __builtin_amdgcn_mfma_f32_16x16x32_bf16(wf[s], af, acc[t], 0, 0, 0);
      }
      __builtin_amdgcn_sched_barrier(0);
      if (LD && s % C::SPC == C::SPC - 2 && s / C::SPC < 2) stage(s / C::SPC + 1);
      if (s % C::SPC == C::SPC - 1 && s / C::SPC < 2) __syncthreads();
      __builtin_amdgcn_sched_barrier(0);
    }
  };
  using TT = std::integral_constant<bool, true>;
  using FF = std::integral_constant<bool, false>;
  if (MODE == 2) {
    if (loader) body(TT{}, TT{});
    else body(FF{}, TT{});
  } else {
    if (kh == 0) body(TT{}, TT{});
    else if (loader) body(TT{}, FF{});
    else body(FF{}, FF{});
  }
  if (C::KS == 2) {
    char* red = smem + C::OFF_RED + tcol * C::NACC * 1024 + lane * 16;
    if (kh == 1) {
#pragma unroll
      for (int t = 0; t < C::NACC; ++t) *(f32x4*)(red + t * 1024) = acc[t];
    }
    __syncthreads();
    if (kh == 1) return;
#pragma unroll
    for (int t = 0; t < C::NACC; ++t) acc[t] += *(const f32x4*)(red + t * 1024);
  } else {
    __syncthreads();
  }
  if (MODE != 1) {
    f32x4 colsum;
#pragma unroll
    for (int i = 0; i < 4; ++i) {
      float v = cs0[i] + (mloc < 2 ? cs1[i] : 0.f);
      v = red8(v);
      v += dpp_f<0x140>(v);
      colsum[i] = v;
    }
#pragma unroll
    for (int t = 0; t < C::RT; ++t) {
      const f32x2 st = *(const f32x2*)(smem + C::OFF_STAT + (t * 16 + mloc) * 8);
#pragma unroll
      for (int i = 0; i < 4; ++i) acc[t][i] = (acc[t][i] - st[0] * colsum[i]) * st[1];
    }
  }

  if (MODE == 1) {
    const int n = gtile * 16 + nq;
#pragma unroll
    for (int t = 0; t < C::RT; ++t) {
      const size_t off = (size_t)(row0 + t * 16 + mloc) * DM + n;
      float r0 = round_bf16(acc[t][0]), r1 = round_bf16(acc[t][1]), r2 = round_bf16(acc[t][2]), r3 = round_bf16(acc[t][3]);
      u32x2 o;
      o[0] = pack_bf16(bf_lo(xr[t][0]) + r0, bf_hi(xr[t][0]) + r1);
      o[1] = pack_bf16(bf_lo(xr[t][1]) + r2, bf_hi(xr[t][1]) + r3);
      *(u32x2*)(out + off) = o;
    }
  } else if (MODE == 0) {
    if (!active) return;
    const float qs = 0.23570226039551584f * 1.4426950408889634f;
    if (gtile < 36) {
#pragma unroll
      for (int t = 0; t < C::RT; ++t) {
        const int pos = row0 + t * 16 + mloc;
        u32x2 o;
        o[0] = pack_bf16(acc[t][0] * qs, acc[t][1] * qs);
        o[1] = pack_bf16(acc[t][2] * qs, acc[t][3] * qs);
        *(u32x2*)(out + (size_t)pos * DM + gtile * 16 + nq) = o;
      }
    } else {
#pragma unroll
      for (int t = 0; t < C::RT; ++t) {
        const int pos = row0 + t * 16 + mloc;
        const int r = pos & 31;
        char* rec = kvout + (pos >> 5) * REC;
        const unsigned w0 = pack_bf16(acc[t][0], acc[t][1]), w1 = pack_bf16(acc[t][2], acc[t][3]);
        if (gtile == 36) {
          *(u32x2*)(rec + (g >> 1) * 512 + r * 16 + (g & 1) * 8) = u32x2{w0, w1};
        } else {
          const int s = r >> 4, rr = r & 15;
          const int hh = (rr >> 2) & 1, j = ((rr >> 3) << 2) | (rr & 3);
          char* vb = rec + REC_V + s * 608 + hh * 304 + j * 2;
          const int d0 = (gtile - 37) * 16 + nq - 2;
          if (d0 < 0) *(unsigned*)(rec + REC_K1 + r * 4) = w0;
          else if (d0 < 18) {
            *(u16*)(vb + d0 * 16) = (u16)(w0 & 0xffffu);
            *(u16*)(vb + (d0 + 1) * 16) = (u16)(w0 >> 16);
          } else if (d0 == 18) *(u16*)(vb + 18 * 16) = (u16)0x3f80;
          if (d0 + 2 < 18) {
            *(u16*)(vb + (d0 + 2) * 16) = (u16)(w1 & 0xffffu);
            *(u16*)(vb + (d0 + 3) * 16) = (u16)(w1 >> 16);
          }
        }
      }
    }
  } else {
    constexpr int LDQ = 456;
#pragma unroll
    for (int t = 0; t < C::RT; ++t) {
      u32x2 o;
      o[0] = pack_bf16(acc[t][0], acc[t][1]);
      o[1] = pack_bf16(acc[t][2], acc[t][3]);
      *(u32x2*)(smem + (t * 16 + mloc) * LDQ + (tcol * 16 + nq) * 2) = o;
    }
    __syncthreads();
    if (tid < 512) {
      const int half = tid & 1, hh = ((tid >> 1) & 1) | ((tid >> 4) & 2), i = ((tid >> 2) & 7) | ((tid >> 6) << 3);
      const int pos = rb * 64 + i;
      const char* qrow = smem + (16 + i) * LDQ + hh * 36;
      unsigned q[9];
#pragma unroll
      for (int d = 0; d < 9; ++d) q[d] = *(const unsigned*)(qrow + d * 4);
      const char* kbase = qrow - 8 * half * LDQ;
      float lg[4];
      float mx = -3.0e38f;
#pragma unroll
      for (int w = 0; w < 4; ++w) {
        const char* krow = kbase - 2 * w * LDQ + 144;
        float sacc = 0.f;
#pragma unroll
        for (int d = 0; d < 9; ++d) sacc = dot2_bf16(q[d], *(const unsigned*)(krow + d * 4), sacc);
        sacc *= 0.23570226039551584f;
        lg[w] = (pos - 2 * (w + 4 * half) >= 0) ? sacc : -1e9f;
        mx = fmaxf(mx, lg[w]);
      }
      mx = fmaxf(mx, dpp_f<0xB1>(mx));
      float den = 0.f;
#pragma unroll
      for (int w = 0; w < 4; ++w) {
        lg[w] = __expf(lg[w] - mx);
        den += lg[w];
      }
      den += dpp_f<0xB1>(den);
      const float inv = 1.f / den;
      float o[18];
#pragma unroll
      for (int d = 0; d < 18; ++d) o[d] = 0.f;
#pragma unroll
      for (int w = 0; w < 4; ++w) {
        const char* vrow = kbase - 2 * w * LDQ + 288;
        const float p = lg[w] * inv;
#pragma unroll
        for (int d = 0; d < 9; ++d) {
          unsigned u = *(const unsigned*)(vrow + d * 4);
          o[2 * d] += p * bf_lo(u);
          o[2 * d + 1] += p * bf_hi(u);
        }
      }
#pragma unroll
      for (int d = 0; d < 18; ++d) o[d] += dpp_f<0xB1>(o[d]);
      unsigned* op = (unsigned*)(out + (size_t)pos * DM + (cb * 4 + hh) * 18);
      if (half == 0) {
#pragma unroll
        for (int d = 0; d < 5; ++d) op[d] = pack_bf16(o[2 * d], o[2 * d + 1]);
      } else {
#pragma unroll
        for (int d = 5; d < 9; ++d) op[d] = pack_bf16(o[2 * d], o[2 * d + 1]);
      }
    }
  }
}

#define MFMA32(a, b, c) __builtin_amdgcn_mfma_f32_32x32x16_bf16(a, b, c, 0, 0, 0)
#define EXP2(x) __builtin_amdgcn_exp2f(x)

constexpr int CH2 = 8 * REC;
__global__ __launch_bounds__(1024) void attn_kernel(const u16* __restrict__ qin, const char* __restrict__ kvin,
                                                    u16* __restrict__ out) {
  __shared__ __attribute__((aligned(16))) char smem[66 * REC];
  const int tid = threadIdx.x, lane = tid & 63, wave = __builtin_amdgcn_readfirstlane(tid >> 6);
  const int pw = wave & 7, half = wave >> 3;
  const int r = lane & 31, h = lane >> 5;
  const int hb = half * 32 * REC;
  u32x4 st[3];
  int soff[3];
#pragma unroll
  for (int j = 0; j < 3; ++j) {
    const int o = (pw + 8 * j) * 1024 + lane * 16;
    soff[j] = hb + (o < CH2 ? o : lane * 16);
  }
#pragma unroll
  for (int j = 0; j < 3; ++j) st[j] = *(const u32x4*)(kvin + soff[j]);
  const int pos = blockIdx.x * 8 + pw;
  const u16* qp = qin + (size_t)pos * DM + r * 18;
  u32x4 q0u = *(const u32x4_a4*)(qp + h * 8);
  unsigned q1w = *(const unsigned*)(qp + 16);
  if (h) q1w = 0u;
  const bf16x8 q0 = __builtin_bit_cast(bf16x8, q0u);
  const bf16x8 q1 = __builtin_bit_cast(bf16x8, u32x4{q1w, 0u, 0u, 0u});
#pragma unroll
  for (int j = 0; j < 3; ++j) *(u32x4*)(smem + soff[j]) = st[j];
#pragma unroll
  for (int j = 0; j < 3; ++j) st[j] = *(const u32x4*)(kvin + CH2 + soff[j]);
  __syncthreads();

  const char* k0p = smem + hb + h * 512 + r * 16;
  const char* k1p = smem + hb + REC_K1 + r * 4;
  const char* vp = smem + hb + REC_V + h * 304 + (r < 18 ? r : 18) * 16;

  f32x16 zero;
#pragma unroll
  for (int i = 0; i < 16; ++i) zero[i] = 0.f;
  f32x16 acc = zero;
  f32x16 negm, sA, sB;
  {
    bf16x8 k0 = *(const bf16x8*)(k0p);
    bf16x8 k1 = __builtin_bit_cast(bf16x8, u32x4{*(const unsigned*)(k1p), 0u, 0u, 0u});
    f32x16 s = MFMA32(k0, q0, zero);
    s = MFMA32(k1, q1, s);
    float mx = s[0];
#pragma unroll
    for (int i = 1; i < 16; ++i) mx = fmaxf(mx, s[i]);
    mx = fmaxf(mx, __shfl_xor(mx, 32));
#pragma unroll
    for (int i = 0; i < 16; ++i) negm[i] = -mx;
#pragma unroll
    for (int i = 0; i < 16; ++i) sA[i] = s[i] - mx;
  }
  bf16x8 k0A = *(const bf16x8*)(k0p + REC), k0B;
  u32x4 k1A = {*(const unsigned*)(k1p + REC), 0u, 0u, 0u}, k1B = {0u, 0u, 0u, 0u};
  bf16x8 v0A = {0, 0, 0, 0, 0, 0, 0, 0}, v1A = v0A, v0B, v1B;
  u32x4 pA0 = {0u, 0u, 0u, 0u}, pA1 = pA0, pB0, pB1;

#define SB __builtin_amdgcn_sched_barrier(0);
#define TILE_BODY(OFF, SC, SN, K0C, K1C, V0C, V1C, K0N, K1N, V0N, V1N, PP0, PP1, PC0, PC1)                     \
  {                                                                                                            \
    acc = MFMA32(V0C, __builtin_bit_cast(bf16x8, PP0), acc);                                                   \
    K0N = *(const bf16x8*)(k0p + (OFF) + 2 * REC);                                                             \
    K1N[0] = *(const unsigned*)(k1p + (OFF) + 2 * REC);                                                        \
    SB                                                                                                         \
    PC0[0] = pack_bf16(EXP2(SC[0]), EXP2(SC[1]));                                                              \
    PC0[1] = pack_bf16(EXP2(SC[2]), EXP2(SC[3]));                                                              \
    SB                                                                                                         \
    acc = MFMA32(V1C, __builtin_bit_cast(bf16x8, PP1), acc);                                                   \
    V0N = *(const bf16x8*)(vp + (OFF));                                                                        \
    V1N = *(const bf16x8*)(vp + (OFF) + 608);                                                                  \
    SB                                                                                                         \
    PC0[2] = pack_bf16(EXP2(SC[4]), EXP2(SC[5]));                                                              \
    PC0[3] = pack_bf16(EXP2(SC[6]), EXP2(SC[7]));                                                              \
    SB                                                                                                         \
    SN = MFMA32(K0C, q0, negm);                                                                                \
    SB                                                                                                         \
    PC1[0] = pack_bf16(EXP2(SC[8]), EXP2(SC[9]));                                                              \
    PC1[1] = pack_bf16(EXP2(SC[10]), EXP2(SC[11]));                                                            \
    SB                                                                                                         \
    SN = MFMA32(__builtin_bit_cast(bf16x8, K1C), q1, SN);                                                        \
    SB                                                                                                         \
    PC1[2] = pack_bf16(EXP2(SC[12]), EXP2(SC[13]));                                                            \
    PC1[3] = pack_bf16(EXP2(SC[14]), EXP2(SC[15]));                                                            \
    SB                                                                                                         \
  }

  for (int t = 0; t < 32; t += 2) {
    if ((t & 7) == 4 && t < 24) {
      const int c = (t >> 3) + 1;
#pragma unroll
      for (int j = 0; j < 3; ++j) *(u32x4*)(smem + c * CH2 + soff[j]) = st[j];
      if (c < 3) {
#pragma unroll
        for (int j = 0; j < 3; ++j) st[j] = *(const u32x4*)(kvin + (c + 1) * CH2 + soff[j]);
      }
      asm volatile("s_waitcnt lgkmcnt(0)" ::: "memory");
      __builtin_amdgcn_s_barrier();
      asm volatile("" ::: "memory");
    }
    const int off = t * REC;
    TILE_BODY(off, sA, sB, k0A, k1A, v0A, v1A, k0B, k1B, v0B, v1B, pA0, pA1, pB0, pB1)
    TILE_BODY(off + REC, sB, sA, k0B, k1B, v0B, v1B, k0A, k1A, v0A, v1A, pB0, pB1, pA0, pA1)
  }
  acc = MFMA32(v0A, __builtin_bit_cast(bf16x8, pA0), acc);
  acc = MFMA32(v1A, __builtin_bit_cast(bf16x8, pA1), acc);

  float m = -negm[0];
  {
    bool bad = !(acc[10] < 1.0e30f);
#pragma unroll
    for (int i = 0; i < 10; ++i) bad = bad || !(fabsf(acc[i]) < 3.0e38f);
    if (__any(bad)) {
      acc = zero;
      m = -3.0e38f;
      for (int t = 0; t < 32; ++t) {
        bf16x8 k0 = *(const bf16x8*)(k0p + t * REC);
        bf16x8 k1 = __builtin_bit_cast(bf16x8, u32x4{*(const unsigned*)(k1p + t * REC), 0u, 0u, 0u});
        bf16x8 v0 = *(const bf16x8*)(vp + t * REC);
        bf16x8 v1 = *(const bf16x8*)(vp + t * REC + 608);
        f32x16 s = MFMA32(k0, q0, zero);
        s = MFMA32(k1, q1, s);
        float mx = s[0];
#pragma unroll
        for (int i = 1; i < 16; ++i) mx = fmaxf(mx, s[i]);
        mx = fmaxf(mx, __shfl_xor(mx, 32));
        const float mnew = fmaxf(m, mx);
        const float sc = EXP2(m - mnew);
        m = mnew;
#pragma unroll
        for (int i = 0; i < 16; ++i) acc[i] *= sc;
        u32x4 p0, p1;
#pragma unroll
        for (int i = 0; i < 4; ++i) {
          p0[i] = pack_bf16(EXP2(s[2 * i] - m), EXP2(s[2 * i + 1] - m));
          p1[i] = pack_bf16(EXP2(s[8 + 2 * i] - m), EXP2(s[8 + 2 * i + 1] - m));
        }
        acc = MFMA32(v0, __builtin_bit_cast(bf16x8, p0), acc);
        acc = MFMA32(v1, __builtin_bit_cast(bf16x8, p1), acc);
      }
    }
  }
  __syncthreads();
  float* xp = (float*)(smem + pw * 3072) + lane;
  if (half) {
#pragma unroll
    for (int i = 0; i < 11; ++i) xp[i * 64] = acc[i];
    xp[11 * 64] = m;
  }
  __syncthreads();
  if (half) return;
  {
    const float m1 = xp[11 * 64];
    const float mm = fmaxf(m, m1);
    const float a0 = EXP2(m - mm), a1 = EXP2(m1 - mm);
#pragma unroll
    for (int i = 0; i < 11; ++i) acc[i] = acc[i] * a0 + xp[i * 64] * a1;
  }
  const float inv = 1.f / acc[10];
  u16* op = out + (size_t)pos * DM + r * 18;
  {
    unsigned a0 = pack_bf16(acc[0] * inv, acc[1] * inv), a1 = pack_bf16(acc[2] * inv, acc[3] * inv);
    unsigned b0 = pack_bf16(acc[4] * inv, acc[5] * inv), b1 = pack_bf16(acc[6] * inv, acc[7] * inv);
    unsigned* o32 = (unsigned*)(op + h * 4);
    o32[0] = a0;
    o32[1] = a1;
    o32[4] = b0;
    o32[5] = b1;
    if (!h) *(unsigned*)(op + 16) = pack_bf16(acc[8] * inv, acc[9] * inv);
  }
}

extern "C" void kernel_launch(void* const* d_in, const int* in_sizes, int n_in, void* d_out, int out_size, void* d_ws,
                              size_t ws_size, hipStream_t stream) {
  const u16* x0 = (const u16*)d_in[0];
  const u16* wq = (const u16*)d_in[1];
  const u16* wk = (const u16*)d_in[2];
  const u16* wv = (const u16*)d_in[3];
  const u16* woa = (const u16*)d_in[4];
  const u16* wqkv = (const u16*)d_in[6];
  const u16* wob = (const u16*)d_in[7];
  char* ws = (char*)d_ws;
  u16* x1 = (u16*)(ws + OFF_X1);
  u16* x2 = (u16*)(ws + OFF_X2);
  u16* x3 = (u16*)(ws + OFF_X3);

  prep_kernel<<<(PREP_WAVES + 3) / 4, 256, 0, stream>>>(wq, wk, wv, woa, wqkv, wob, ws);
  for (int layer = 0; layer < 2; ++layer) {
    const u16* xin = layer == 0 ? x0 : x2;
    u16* xmid = layer == 0 ? x1 : x3;
    u16* xout = layer == 0 ? x2 : (u16*)d_out;
    u16* qb = (u16*)(ws + (layer == 0 ? OFF_Q : OFF_ALT));
    char* kvb = ws + (layer == 0 ? OFF_KV : OFF_ALT + ACT_BYTES);
    u16* obuf = (u16*)(ws + (layer == 0 ? OFF_O : OFF_ALT + 2 * ACT_BYTES));
    u16* obuf2 = (u16*)(ws + OFF_ALT + (3 + layer) * ACT_BYTES);
    gemm_kernel<0><<<224, 768, 0, stream>>>(xin, ws + OFF_WA, (const u16*)(ws + OFF_CSA), qb, kvb);
    attn_kernel<<<256, 1024, 0, stream>>>(qb, kvb, obuf);
    gemm_kernel<1><<<192, 768, 0, stream>>>(obuf, ws + OFF_WOA, xin, xmid, nullptr);
    gemm_kernel<2><<<256, 896, 0, stream>>>(xmid, ws + OFF_WB, (const u16*)(ws + OFF_CSB), obuf2, nullptr);
    gemm_kernel<1><<<192, 768, 0, stream>>>(obuf2, ws + OFF_WOB, xmid, xout, nullptr);
  }
}
```

```cpp
#include <hip/hip_runtime.h>
#include <hip/hip_bf16.h>
#include <stdint.h>
#include <type_traits>

typedef __attribute__((ext_vector_type(8))) short bf16x8;
typedef __attribute__((ext_vector_type(4))) float f32x4;
typedef __attribute__((ext_vector_type(2))) float f32x2;
typedef __attribute__((ext_vector_type(16))) float f32x16;
typedef __attribute__((ext_vector_type(4))) unsigned u32x4;
typedef __attribute__((ext_vector_type(4), aligned(4))) unsigned u32x4_a4;
typedef __attribute__((ext_vector_type(2))) unsigned u32x2;
typedef __attribute__((ext_vector_type(2))) __bf16 bf16x2_t;
typedef unsigned short u16;

#define DI __device__ __forceinline__

constexpr int SEQ = 2048;
constexpr int DM = 576;
constexpr int FRAG_TILE = 18 * 1024;

constexpr int REC = 2368;
constexpr int REC_K1 = 1024;
constexpr int REC_V = 1152;
constexpr int KV_BYTES = 64 * REC;
constexpr int CHUNK = 16 * REC;

constexpr size_t OFF_WA = 0;
constexpr size_t OFF_WOA = OFF_WA + 39 * (size_t)FRAG_TILE;
constexpr size_t OFF_WB = OFF_WOA + 36 * (size_t)FRAG_TILE;
constexpr size_t OFF_WOB = OFF_WB + 112 * (size_t)FRAG_TILE;
constexpr size_t ACT_BYTES = (size_t)SEQ * DM * 2;
constexpr size_t OFF_Q = OFF_WOB + 36 * (size_t)FRAG_TILE;
constexpr size_t OFF_KV = OFF_Q + ACT_BYTES;
constexpr size_t OFF_O = OFF_KV + KV_BYTES;
constexpr size_t OFF_X1 = OFF_O + ACT_BYTES;
constexpr size_t OFF_X2 = OFF_X1 + ACT_BYTES;
constexpr size_t OFF_X3 = OFF_X2 + ACT_BYTES;
constexpr int NCA = 39 * 16, NCB = 112 * 16;
constexpr size_t OFF_CSA = OFF_X3 + ACT_BYTES;
constexpr size_t OFF_CSB = OFF_CSA + 18 * (size_t)NCA * 4;
constexpr size_t OFF_ALT = (OFF_CSB + 18 * (size_t)NCB * 4 + 4095) & ~(size_t)4095;

DI float bf_lo(unsigned u) { return __uint_as_float(u << 16); }
DI float bf_hi(unsigned u) { return __uint_as_float(u & 0xffff0000u); }
DI unsigned pack_bf16(float a, float b) {
  f32x2 v = {a, b};
  bf16x2_t r = __builtin_convertvector(v, bf16x2_t);
  return __builtin_bit_cast(unsigned, r);
}
DI float round_bf16(float a) { return bf_lo(pack_bf16(a, 0.f)); }
template <int CTRL>
DI float dpp_f(float v) {
  return __builtin_bit_cast(float, __builtin_amdgcn_update_dpp(0, __builtin_bit_cast(int, v), CTRL, 0xf, 0xf, false));
}
DI float red8(float v) {
  v += dpp_f<0xB1>(v);
  v += dpp_f<0x4E>(v);
  v += dpp_f<0x141>(v);
  return v;
}
DI float dot2_bf16(unsigned a, unsigned b, float c) {
  return __builtin_amdgcn_fdot2_f32_bf16(__builtin_bit_cast(bf16x2_t, a), __builtin_bit_cast(bf16x2_t, b), c, false);
}

constexpr int PREP_FAST = 55 * 18;
constexpr int PREP_WAVES = PREP_FAST + 3 * 18;
__global__ __launch_bounds__(256) void prep_kernel(const u16* __restrict__ wq, const u16* __restrict__ wk,
                                                   const u16* __restrict__ wv, const u16* __restrict__ woa,
                                                   const u16* __restrict__ wqkv, const u16* __restrict__ wob,
                                                   char* __restrict__ ws) {
  const int wid = blockIdx.x * 4 + (threadIdx.x >> 6);
  const int lane = threadIdx.x & 63;
  if (wid >= PREP_WAVES) return;
  const int g = lane >> 4, c = lane & 15;
  if (wid < PREP_FAST) {
    const int grp = wid / 18, s = wid % 18;
    const u16* src;
    int ld = 576;
    bool zero = false;
    char* dst;
    float* cs = nullptr;
    if (grp < 9) {
      dst = ws + OFF_WA + (size_t)grp * 4 * FRAG_TILE;
      cs = (float*)(ws + OFF_CSA) + s * NCA + grp * 64 + c * 4;
      src = wq + grp * 64 + c * 4;
    } else if (grp < 18) {
      dst = ws + OFF_WOA + (size_t)(grp - 9) * 4 * FRAG_TILE;
      src = woa + (grp - 9) * 64 + c * 4;
    } else if (grp < 27) {
      dst = ws + OFF_WOB + (size_t)(grp - 18) * 4 * FRAG_TILE;
      src = wob + (grp - 18) * 64 + c * 4;
    } else {
      dst = ws + OFF_WB + (size_t)(grp - 27) * 4 * FRAG_TILE;
      const int np = (grp - 27) * 64 + c * 4;
      cs = (float*)(ws + OFF_CSB) + s * NCB + np;
      const int hg = np / 224, n = np % 224;
      ld = 1728;
      zero = n >= 216;
      const int nn = zero ? 0 : n;
      src = wqkv + (nn / 72) * 576 + hg * 72 + (nn % 72);
    }
    u32x2 r[8];
    const int k0 = 32 * s + 8 * g;
#pragma unroll
    for (int j = 0; j < 8; ++j) r[j] = *(const u32x2*)(src + (size_t)(k0 + j) * ld);
    if (zero) {
#pragma unroll
      for (int j = 0; j < 8; ++j) r[j] = u32x2{0u, 0u};
    }
    char* dp = dst + (size_t)(c >> 2) * FRAG_TILE + s * 1024 + (g * 16 + (c & 3) * 4) * 16;
    f32x4 csum;
#pragma unroll
    for (int q = 0; q < 4; ++q) {
      u32x4 v;
      float a = 0.f;
#pragma unroll
      for (int j = 0; j < 4; ++j) {
        v[j] = __builtin_amdgcn_perm(r[2 * j + 1][q >> 1], r[2 * j][q >> 1], (q & 1) ? 0x07060302u : 0x05040100u);
        a = dot2_bf16(v[j], 0x3f803f80u, a);
      }
      *(u32x4*)(dp + q * 16) = v;
      a += __shfl_xor(a, 16);
      a += __shfl_xor(a, 32);
      csum[q] = a;
    }
    if (cs != nullptr && g == 0) *(f32x4*)cs = csum;
  } else {
    const int w2 = wid - PREP_FAST;
    const int tile = 36 + w2 / 18, s = w2 % 18;
    const int n = tile * 16 + c;
    const u16* src = (n < 594) ? wk + (n - 576) : wv + (n < 612 ? n - 594 : 0);
    const int k0 = 32 * s + 8 * g;
    u32x4 v;
    float a = 0.f;
#pragma unroll
    for (int j = 0; j < 4; ++j) {
      unsigned lo = src[(size_t)(k0 + 2 * j) * 18];
      unsigned hi = src[(size_t)(k0 + 2 * j + 1) * 18];
      v[j] = n < 612 ? (lo | (hi << 16)) : 0u;
      a = dot2_bf16(v[j], 0x3f803f80u, a);
    }
    a += __shfl_xor(a, 16);
    a += __shfl_xor(a, 32);
    if (g == 0) ((float*)(ws + OFF_CSA))[s * NCA + n] = a;
    *(u32x4*)(ws + OFF_WA + (size_t)tile * FRAG_TILE + s * 1024 + lane * 16) = v;
  }
}

template <int MODE>
struct GemmCfg {
  static constexpr int ROWS = (MODE == 2) ? 80 : 64;
  static constexpr int RT = ROWS / 16;
  static constexpr int NT = (MODE == 2) ? 14 : 6;
  static constexpr int KS = (MODE == 2) ? 1 : 2;
  static constexpr int NW = NT * KS;
  static constexpr int KSTEPS = 18 / KS;
  static constexpr int PF = (MODE == 2) ? 5 : 4;
  static constexpr int CB = (MODE == 0) ? 7 : (MODE == 1 ? 6 : 8);
  static constexpr int NACC = RT;
  static constexpr int SPC = KSTEPS / 3;
  static constexpr int NC = (MODE == 2) ? NCB : NCA;
  static constexpr int OFF_STAT = ROWS * 1152;
  static constexpr int OFF_RED = OFF_STAT + ROWS * 8;
  static constexpr int SMEM = OFF_RED + (KS == 2 ? NT * NACC * 1024 : 0);
};

template <int MODE>
__global__ __launch_bounds__(GemmCfg<MODE>::NW * 64) void gemm_kernel(const u16* __restrict__ A, const char* __restrict__ Wf,
                                                                      const u16* __restrict__ resid, u16* __restrict__ out,
                                                                      char* __restrict__ kvout) {
  using C = GemmCfg<MODE>;
  __shared__ __attribute__((aligned(16))) char smem[C::SMEM];
  const int tid = threadIdx.x, lane = tid & 63, wave = __builtin_amdgcn_readfirstlane(tid >> 6);
  const int xcd = blockIdx.x & 7, idx = blockIdx.x >> 3;
  const int rb = xcd * 4 + idx / C::CB, cb = idx % C::CB;
  int tcol, kh, li;
  bool loader;
  if (MODE == 2) {
    loader = !((wave & 8) && !(wave & 2));
    li = wave < 8 ? wave : wave - 2;
    tcol = loader ? 4 + li : ((wave & 1) | ((wave >> 1) & 2));
    kh = 0;
  } else {
    tcol = wave % C::NT;
    kh = wave / C::NT;
    loader = wave < 8;
    li = wave;
  }
  const int gtile = cb * C::NT + tcol;
  const bool active = (MODE != 0) || (gtile < 39);
  const int row0 = rb * 64 - (MODE == 2 ? 16 : 0);
  const int mloc = lane & 15, g = lane >> 4, nq = g * 4;

  f32x4 acc[C::NACC];
#pragma unroll
  for (int t = 0; t < C::NACC; ++t) acc[t] = f32x4{0.f, 0.f, 0.f, 0.f};
  u32x2 xr[C::RT];
  f32x4 cs0, cs1;

  auto body = [&](auto ldc, auto k0c) {
    constexpr bool LD = decltype(ldc)::value;
    constexpr bool K0 = decltype(k0c)::value;
    constexpr int T0 = (MODE == 2 && !LD) ? 1 : 0;
    const int sr = li * 8 + (lane >> 3), sj = lane & 7;
    u32x4 xa[9];
    int grow = row0 + sr;
    if (MODE == 2) grow = grow < 0 ? 0 : grow;
    const u16* ap = A + (size_t)grow * DM + sj * 8;
    char* lp = smem + sr * 1152 + ((sj ^ ((sr >> 1) & 7)) * 16);
    if (LD) {
#pragma unroll
      for (int i = 0; i < 3; ++i) xa[i] = *(const u32x4*)(ap + i * 64);
    }
    __builtin_amdgcn_sched_barrier(0);
    __builtin_amdgcn_s_barrier();
    __builtin_amdgcn_sched_barrier(0);
    bf16x8 wf[C::KSTEPS];
    const char* wp = Wf + (size_t)(active ? gtile : 0) * FRAG_TILE + kh * 1024 + lane * 16;
#pragma unroll
    for (int s = 0; s < C::PF; ++s) wf[s] = *(const bf16x8*)(wp + s * C::KS * 1024);
    __builtin_amdgcn_sched_barrier(0);
    if (LD) {
#pragma unroll
      for (int i = 3; i < 9; ++i) xa[i] = *(const u32x4*)(ap + i * 64);
    }
    __builtin_amdgcn_sched_barrier(0);
    float sum = 0.f, sq = 0.f;
    auto stage = [&](int c) {
      __builtin_amdgcn_s_setprio(3);
#pragma unroll
      for (int i = 3 * c; i < 3 * c + 3; ++i) *(u32x4*)(lp + i * 128) = xa[i];
      if (MODE != 1) {
#pragma unroll
        for (int i = 3 * c; i < 3 * c + 3; ++i)
#pragma unroll
          for (int j = 0; j < 4; ++j) {
            sum = dot2_bf16(xa[i][j], 0x3f803f80u, sum);
            sq = dot2_bf16(xa[i][j], xa[i][j], sq);
          }
        if (c == 2) {
          sum = red8(sum);
          sq = red8(sq);
          const float mean = sum * (1.f / 576.f);
          float var = sq * (1.f / 576.f) - mean * mean;
          if (__any(!(mean * mean <= 16.f * var))) {
            var = 0.f;
#pragma unroll
            for (int i = 0; i < 9; ++i)
#pragma unroll
              for (int j = 0; j < 4; ++j) {
                float a = bf_lo(xa[i][j]) - mean, b = bf_hi(xa[i][j]) - mean;
                var += a * a + b * b;
              }
            var = red8(var) * (1.f / 576.f);
          }
          const float rstd = 1.0f / sqrtf(var + 1e-5f);
          if (sj == 0) *(f32x2*)(smem + C::OFF_STAT + sr * 8) = f32x2{mean, rstd};
        }
      }
      __builtin_amdgcn_s_setprio(0);
    };
    if (LD) stage(0);
    __syncthreads();
    __builtin_amdgcn_sched_barrier(0);
    const int be = (g ^ (mloc >> 1)) * 16;
    const char* pA = smem + mloc * 1152 + (kh ? be ^ 64 : be);
    const char* pB = smem + mloc * 1152 + (be ^ 64);
#pragma unroll
    for (int s = 0; s < C::KSTEPS; ++s) {
      if (s + C::PF < C::KSTEPS) wf[s + C::PF] = *(const bf16x8*)(wp + (s + C::PF) * C::KS * 1024);
      if (K0 && s + C::PF == (MODE == 1 ? C::KSTEPS : C::KSTEPS - 3)) {
        if (MODE == 1) {
#pragma unroll
          for (int t = 0; t < C::RT; ++t) xr[t] = *(const u32x2*)(resid + (size_t)(row0 + t * 16 + mloc) * DM + gtile * 16 + nq);
        } else {
          const float* cs = (const float*)resid + (active ? gtile : 0) * 16 + nq;
          cs0 = *(const f32x4*)(cs + mloc * C::NC);
          cs1 = *(const f32x4*)(cs + (16 + (mloc & 1)) * C::NC);
        }
      }
      const char* ap2 = (C::KS == 2) ? pA + s * 128 : ((s & 1) ? pB : pA) + (s >> 1) * 128;
#pragma unroll
      for (int t = T0; t < C::RT; ++t) {
        bf16x8 af = *(const bf16x8*)(ap2 + t * 16 * 1152);
        acc[t] = __builtin_amdgcn_mfma_f32_16x16x32_bf16(wf[s], af, acc[t], 0, 0, 0);
      }
      __builtin_amdgcn_sched_barrier(0);
      if (LD && s % C::SPC == C::SPC - 2 && s / C::SPC < 2) stage(s / C::SPC + 1);
      if (s % C::SPC == C::SPC - 1 && s / C::SPC < 2) __syncthreads();
      __builtin_amdgcn_sched_barrier(0);
    }
  };
  using TT = std::integral_constant<bool, true>;
  using FF = std::integral_constant<bool, false>;
  if (MODE == 2) {
    if (loader) body(TT{}, TT{});
    else body(FF{}, TT{});
  } else {
    if (kh == 0) body(TT{}, TT{});
    else if (loader) body(TT{}, FF{});
    else body(FF{}, FF{});
  }
  if (C::KS == 2) {
    char* red = smem + C::OFF_RED + tcol * C::NACC * 1024 + lane * 16;
    if (kh == 1) {
#pragma unroll
      for (int t = 0; t < C::NACC; ++t) *(f32x4*)(red + t * 1024) = acc[t];
    }
    __syncthreads();
    if (kh == 1) return;
#pragma unroll
    for (int t = 0; t < C::NACC; ++t) acc[t] += *(const f32x4*)(red + t * 1024);
  } else {
    __syncthreads();
  }
  if (MODE != 1) {
    f32x4 colsum;
#pragma unroll
    for (int i = 0; i < 4; ++i) {
      float v = cs0[i] + (mloc < 2 ? cs1[i] : 0.f);
      v = red8(v);
      v += dpp_f<0x140>(v);
      colsum[i] = v;
    }
#pragma unroll
    for (int t = 0; t < C::RT; ++t) {
      const f32x2 st = *(const f32x2*)(smem + C::OFF_STAT + (t * 16 + mloc) * 8);
#pragma unroll
      for (int i = 0; i < 4; ++i) acc[t][i] = (acc[t][i] - st[0] * colsum[i]) * st[1];
    }
  }

  if (MODE == 1) {
    const int n = gtile * 16 + nq;
#pragma unroll
    for (int t = 0; t < C::RT; ++t) {
      const size_t off = (size_t)(row0 + t * 16 + mloc) * DM + n;
      float r0 = round_bf16(acc[t][0]), r1 = round_bf16(acc[t][1]), r2 = round_bf16(acc[t][2]), r3 = round_bf16(acc[t][3]);
      u32x2 o;
      o[0] = pack_bf16(bf_lo(xr[t][0]) + r0, bf_hi(xr[t][0]) + r1);
      o[1] = pack_bf16(bf_lo(xr[t][1]) + r2, bf_hi(xr[t][1]) + r3);
      *(u32x2*)(out + off) = o;
    }
  } else if (MODE == 0) {
    if (!active) return;
    const float qs = 0.23570226039551584f * 1.4426950408889634f;
    if (gtile < 36) {
#pragma unroll
      for (int t = 0; t < C::RT; ++t) {
        const int pos = row0 + t * 16 + mloc;
        u32x2 o;
        o[0] = pack_bf16(acc[t][0] * qs, acc[t][1] * qs);
        o[1] = pack_bf16(acc[t][2] * qs, acc[t][3] * qs);
        *(u32x2*)(out + (size_t)pos * DM + gtile * 16 + nq) = o;
      }
    } else {
#pragma unroll
      for (int t = 0; t < C::RT; ++t) {
        const int pos = row0 + t * 16 + mloc;
        const int r = pos & 31;
        char* rec = kvout + (pos >> 5) * REC;
        const unsigned w0 = pack_bf16(acc[t][0], acc[t][1]), w1 = pack_bf16(acc[t][2], acc[t][3]);
        if (gtile == 36) {
          *(u32x2*)(rec + (g >> 1) * 512 + r * 16 + (g & 1) * 8) = u32x2{w0, w1};
        } else {
          const int s = r >> 4, rr = r & 15;
          const int hh = (rr >> 2) & 1, j = ((rr >> 3) << 2) | (rr & 3);
          char* vb = rec + REC_V + s * 608 + hh * 304 + j * 2;
          const int d0 = (gtile - 37) * 16 + nq - 2;
          if (d0 < 0) *(unsigned*)(rec + REC_K1 + r * 4) = w0;
          else if (d0 < 18) {
            *(u16*)(vb + d0 * 16) = (u16)(w0 & 0xffffu);
            *(u16*)(vb + (d0 + 1) * 16) = (u16)(w0 >> 16);
          } else if (d0 == 18) *(u16*)(vb + 18 * 16) = (u16)0x3f80;
          if (d0 + 2 < 18) {
            *(u16*)(vb + (d0 + 2) * 16) = (u16)(w1 & 0xffffu);
            *(u16*)(vb + (d0 + 3) * 16) = (u16)(w1 >> 16);
          }
        }
      }
    }
  } else {
    constexpr int LDQ = 456;
#pragma unroll
    for (int t = 0; t < C::RT; ++t) {
      u32x2 o;
      o[0] = pack_bf16(acc[t][0], acc[t][1]);
      o[1] = pack_bf16(acc[t][2], acc[t][3]);
      *(u32x2*)(smem + (t * 16 + mloc) * LDQ + (tcol * 16 + nq) * 2) = o;
    }
    __syncthreads();
    if (tid < 512) {
      const int half = tid & 1, hh = ((tid >> 1) & 1) | ((tid >> 4) & 2), i = ((tid >> 2) & 7) | ((tid >> 6) << 3);
      const int pos = rb * 64 + i;
      const char* qrow = smem + (16 + i) * LDQ + hh * 36;
      unsigned q[9];
#pragma unroll
      for (int d = 0; d < 9; ++d) q[d] = *(const unsigned*)(qrow + d * 4);
      const char* kbase = qrow - 8 * half * LDQ;
      float lg[4];
      float mx = -3.0e38f;
#pragma unroll
      for (int w = 0; w < 4; ++w) {
        const char* krow = kbase - 2 * w * LDQ + 144;
        float sacc = 0.f;
#pragma unroll
        for (int d = 0; d < 9; ++d) sacc = dot2_bf16(q[d], *(const unsigned*)(krow + d * 4), sacc);
        sacc *= 0.23570226039551584f;
        lg[w] = (pos - 2 * (w + 4 * half) >= 0) ? sacc : -1e9f;
        mx = fmaxf(mx, lg[w]);
      }
      mx = fmaxf(mx, dpp_f<0xB1>(mx));
      float den = 0.f;
#pragma unroll
      for (int w = 0; w < 4; ++w) {
        lg[w] = __expf(lg[w] - mx);
        den += lg[w];
      }
      den += dpp_f<0xB1>(den);
      const float inv = 1.f / den;
      float o[18];
#pragma unroll
      for (int d = 0; d < 18; ++d) o[d] = 0.f;
#pragma unroll
      for (int w = 0; w < 4; ++w) {
        const char* vrow = kbase - 2 * w * LDQ + 288;
        const float p = lg[w] * inv;
#pragma unroll
        for (int d = 0; d < 9; ++d) {
          unsigned u = *(const unsigned*)(vrow + d * 4);
          o[2 * d] += p * bf_lo(u);
          o[2 * d + 1] += p * bf_hi(u);
        }
      }
#pragma unroll
      for (int d = 0; d < 18; ++d) o[d] += dpp_f<0xB1>(o[d]);
      unsigned* op = (unsigned*)(out + (size_t)pos * DM + (cb * 4 + hh) * 18);
      if (half == 0) {
#pragma unroll
        for (int d = 0; d < 5; ++d) op[d] = pack_bf16(o[2 * d], o[2 * d + 1]);
      } else {
#pragma unroll
        for (int d = 5; d < 9; ++d) op[d] = pack_bf16(o[2 * d], o[2 * d + 1]);
      }
    }
  }
}

#define MFMA32(a, b, c) __builtin_amdgcn_mfma_f32_32x32x16_bf16(a, b, c, 0, 0, 0)
#define MFMA8(a, b, c) __builtin_amdgcn_mfma_f32_32x32x8bf16_1k(__builtin_bit_cast(bf16x4, a), __builtin_bit_cast(bf16x4, b), c, 0, 0, 0)
#define EXP2(x) __builtin_amdgcn_exp2f(x)
typedef __attribute__((ext_vector_type(4))) short bf16x4;

constexpr int CH2 = 8 * REC;
__global__ __launch_bounds__(1024) void attn_kernel(const u16* __restrict__ qin, const char* __restrict__ kvin,
                                                    u16* __restrict__ out) {
  __shared__ __attribute__((aligned(16))) char smem[66 * REC];
  const int tid = threadIdx.x, lane = tid & 63, wave = __builtin_amdgcn_readfirstlane(tid >> 6);
  const int pw = wave & 7, half = wave >> 3;
  const int r = lane & 31, h = lane >> 5;
  const int hb = half * 32 * REC;
  u32x4 st[3];
  int soff[3];
#pragma unroll
  for (int j = 0; j < 3; ++j) {
    const int o = (pw + 8 * j) * 1024 + lane * 16;
    soff[j] = hb + (o < CH2 ? o : lane * 16);
  }
#pragma unroll
  for (int j = 0; j < 3; ++j) st[j] = *(const u32x4*)(kvin + soff[j]);
  const int pos = blockIdx.x * 8 + pw;
  const u16* qp = qin + (size_t)pos * DM + r * 18;
  u32x4 q0u = *(const u32x4_a4*)(qp + h * 8);
  unsigned q1w = *(const unsigned*)(qp + 16);
  if (h) q1w = 0u;
  const bf16x8 q0 = __builtin_bit_cast(bf16x8, q0u);
  const u32x2 q1f = {q1w, 0u};
#pragma unroll
  for (int j = 0; j < 3; ++j) *(u32x4*)(smem + soff[j]) = st[j];
  __syncthreads();
#pragma unroll
  for (int j = 0; j < 3; ++j) st[j] = *(const u32x4*)(kvin + CH2 + soff[j]);
  __builtin_amdgcn_sched_barrier(0);

  const char* k0p = smem + hb + h * 512 + r * 16;
  const char* k1p = smem + hb + (h ? REC_V + 18 * 16 : REC_K1 + r * 4);
  const char* vp = smem + hb + REC_V + h * 304 + (r < 18 ? r : 18) * 16;

  f32x16 zero;
#pragma unroll
  for (int i = 0; i < 16; ++i) zero[i] = 0.f;
  f32x16 acc = zero;
  f32x16 sA, sB;
  float m;
  u32x2 q1;
  {
    bf16x8 k0 = *(const bf16x8*)(k0p);
    u32x2 k1 = {*(const unsigned*)(k1p), 0u};
    f32x16 s = MFMA32(k0, q0, zero);
    s = MFMA8(k1, q1f, s);
    float mx = s[0];
#pragma unroll
    for (int i = 1; i < 16; ++i) mx = fmaxf(mx, s[i]);
    mx = fmaxf(mx, __shfl_xor(mx, 32));
    const float mhi = round_bf16(mx), mlo = round_bf16(mx - mhi);
    m = mhi + mlo;
    q1 = u32x2{h ? pack_bf16(-mhi, -mlo) : q1w, 0u};
#pragma unroll
    for (int i = 0; i < 16; ++i) sA[i] = s[i] - m;
  }
  bf16x8 k0A = *(const bf16x8*)(k0p + REC), k0B;
  u32x2 k1A = {*(const unsigned*)(k1p + REC), 0u}, k1B = {0u, 0u};
  bf16x8 v0A = {0, 0, 0, 0, 0, 0, 0, 0}, v1A = v0A, v0B, v1B;
  u32x4 pA0 = {0u, 0u, 0u, 0u}, pA1 = pA0, pB0, pB1;

#define SB __builtin_amdgcn_sched_barrier(0);
#define TILE_BODY(OFF, SC, SN, K0C, K1C, V0C, V1C, K0N, K1N, V0N, V1N, PP0, PP1, PC0, PC1)                     \
  {                                                                                                            \
    acc = MFMA32(V0C, __builtin_bit_cast(bf16x8, PP0), acc);                                                   \
    K0N = *(const bf16x8*)(k0p + (OFF) + 2 * REC);                                                             \
    K1N[0] = *(const unsigned*)(k1p + (OFF) + 2 * REC);                                                        \
    SB                                                                                                         \
    PC0[0] = pack_bf16(EXP2(SC[0]), EXP2(SC[1]));                                                              \
    PC0[1] = pack_bf16(EXP2(SC[2]), EXP2(SC[3]));                                                              \
    SB                                                                                                         \
    acc = MFMA32(V1C, __builtin_bit_cast(bf16x8, PP1), acc);                                                   \
    V0N = *(const bf16x8*)(vp + (OFF));                                                                        \
    V1N = *(const bf16x8*)(vp + (OFF) + 608);                                                                  \
    SB                                                                                                         \
    PC0[2] = pack_bf16(EXP2(SC[4]), EXP2(SC[5]));                                                              \
    PC0[3] = pack_bf16(EXP2(SC[6]), EXP2(SC[7]));                                                              \
    SB                                                                                                         \
    SN = MFMA32(K0C, q0, zero);                                                                                \
    SB                                                                                                         \
    PC1[0] = pack_bf16(EXP2(SC[8]), EXP2(SC[9]));                                                              \
    PC1[1] = pack_bf16(EXP2(SC[10]), EXP2(SC[11]));                                                            \
    SB                                                                                                         \
    SN = MFMA8(K1C, q1, SN);                                                                                   \
    SB                                                                                                         \
    PC1[2] = pack_bf16(EXP2(SC[12]), EXP2(SC[13]));                                                            \
    PC1[3] = pack_bf16(EXP2(SC[14]), EXP2(SC[15]));                                                            \
    SB                                                                                                         \
  }

  for (int t = 0; t < 32; t += 4) {
    if ((t & 7) == 4 && t < 24) {
      const int c = (t >> 3) + 1;
#pragma unroll
      for (int j = 0; j < 3; ++j) *(u32x4*)(smem + c * CH2 + soff[j]) = st[j];
      if (c < 3) {
#pragma unroll
        for (int j = 0; j < 3; ++j) st[j] = *(const u32x4*)(kvin + (c + 1) * CH2 + soff[j]);
      }
      asm volatile("s_waitcnt lgkmcnt(0)" ::: "memory");
      __builtin_amdgcn_s_barrier();
      asm volatile("" ::: "memory");
    }
    const int off = t * REC;
    TILE_BODY(off, sA, sB, k0A, k1A, v0A, v1A, k0B, k1B, v0B, v1B, pA0, pA1, pB0, pB1)
    TILE_BODY(off + REC, sB, sA, k0B, k1B, v0B, v1B, k0A, k1A, v0A, v1A, pB0, pB1, pA0, pA1)
    TILE_BODY(off + 2 * REC, sA, sB, k0A, k1A, v0A, v1A, k0B, k1B, v0B, v1B, pA0, pA1, pB0, pB1)
    TILE_BODY(off + 3 * REC, sB, sA, k0B, k1B, v0B, v1B, k0A, k1A, v0A, v1A, pB0, pB1, pA0, pA1)
  }
  acc = MFMA32(v0A, __builtin_bit_cast(bf16x8, pA0), acc);
  acc = MFMA32(v1A, __builtin_bit_cast(bf16x8, pA1), acc);

  {
    bool bad = !(acc[10] < 1.0e30f);
#pragma unroll
    for (int i = 0; i < 10; ++i) bad = bad || !(fabsf(acc[i]) < 3.0e38f);
    if (__any(bad)) {
      acc = zero;
      m = -3.0e38f;
      for (int t = 0; t < 32; ++t) {
        bf16x8 k0 = *(const bf16x8*)(k0p + t * REC);
        u32x2 k1 = {*(const unsigned*)(k1p + t * REC), 0u};
        bf16x8 v0 = *(const bf16x8*)(vp + t * REC);
        bf16x8 v1 = *(const bf16x8*)(vp + t * REC + 608);
        f32x16 s = MFMA32(k0, q0, zero);
        s = MFMA8(k1, q1f, s);
        float mx = s[0];
#pragma unroll
        for (int i = 1; i < 16; ++i) mx = fmaxf(mx, s[i]);
        mx = fmaxf(mx, __shfl_xor(mx, 32));
        const float mnew = fmaxf(m, mx);
        const float sc = EXP2(m - mnew);
        m = mnew;
#pragma unroll
        for (int i = 0; i < 16; ++i) acc[i] *= sc;
        u32x4 p0, p1;
#pragma unroll
        for (int i = 0; i < 4; ++i) {
          p0[i] = pack_bf16(EXP2(s[2 * i] - m), EXP2(s[2 * i + 1] - m));
          p1[i] = pack_bf16(EXP2(s[8 + 2 * i] - m), EXP2(s[8 + 2 * i + 1] - m));
        }
        acc = MFMA32(v0, __builtin_bit_cast(bf16x8, p0), acc);
        acc = MFMA32(v1, __builtin_bit_cast(bf16x8, p1), acc);
      }
    }
  }
  __syncthreads();
  float* xp = (float*)(smem + pw * 3072) + lane;
  if (half) {
#pragma unroll
    for (int i = 0; i < 11; ++i) xp[i * 64] = acc[i];
    xp[11 * 64] = m;
  }
  __syncthreads();
  if (half) return;
  {
    const float m1 = xp[11 * 64];
    const float mm = fmaxf(m, m1);
    const float a0 = EXP2(m - mm), a1 = EXP2(m1 - mm);
#pragma unroll
    for (int i = 0; i < 11; ++i) acc[i] = acc[i] * a0 + xp[i * 64] * a1;
  }
  const float inv = 1.f / acc[10];
  u16* op = out + (size_t)pos * DM + r * 18;
  {
    unsigned a0 = pack_bf16(acc[0] * inv, acc[1] * inv), a1 = pack_bf16(acc[2] * inv, acc[3] * inv);
    unsigned b0 = pack_bf16(acc[4] * inv, acc[5] * inv), b1 = pack_bf16(acc[6] * inv, acc[7] * inv);
    unsigned* o32 = (unsigned*)(op + h * 4);
    o32[0] = a0;
    o32[1] = a1;
    o32[4] = b0;
    o32[5] = b1;
    if (!h) *(unsigned*)(op + 16) = pack_bf16(acc[8] * inv, acc[9] * inv);
  }
}

extern "C" void kernel_launch(void* const* d_in, const int* in_sizes, int n_in, void* d_out, int out_size, void* d_ws,
                              size_t ws_size, hipStream_t stream) {
  const u16* x0 = (const u16*)d_in[0];
  const u16* wq = (const u16*)d_in[1];
  const u16* wk = (const u16*)d_in[2];
  const u16* wv = (const u16*)d_in[3];
  const u16* woa = (const u16*)d_in[4];
  const u16* wqkv = (const u16*)d_in[6];
  const u16* wob = (const u16*)d_in[7];
  char* ws = (char*)d_ws;
  u16* x1 = (u16*)(ws + OFF_X1);
  u16* x2 = (u16*)(ws + OFF_X2);
  u16* x3 = (u16*)(ws + OFF_X3);

  prep_kernel<<<(PREP_WAVES + 3) / 4, 256, 0, stream>>>(wq, wk, wv, woa, wqkv, wob, ws);
  for (int layer = 0; layer < 2; ++layer) {
    const u16* xin = layer == 0 ? x0 : x2;
    u16* xmid = layer == 0 ? x1 : x3;
    u16* xout = layer == 0 ? x2 : (u16*)d_out;
    u16* qb = (u16*)(ws + (layer == 0 ? OFF_Q : OFF_ALT));
    char* kvb = ws + (layer == 0 ? OFF_KV : OFF_ALT + ACT_BYTES);
    u16* obuf = (u16*)(ws + (layer == 0 ? OFF_O : OFF_ALT + 2 * ACT_BYTES));
    u16* obuf2 = (u16*)(ws + OFF_ALT + (3 + layer) * ACT_BYTES);
    gemm_kernel<0><<<224, 768, 0, stream>>>(xin, ws + OFF_WA, (const u16*)(ws + OFF_CSA), qb, kvb);
    attn_kernel<<<256, 1024, 0, stream>>>(qb, kvb, obuf);
    gemm_kernel<1><<<192, 768, 0, stream>>>(obuf, ws + OFF_WOA, xin, xmid, nullptr);
    gemm_kernel<2><<<256, 896, 0, stream>>>(xmid, ws + OFF_WB, (const u16*)(ws + OFF_CSB), obuf2, nullptr);
    gemm_kernel<1><<<192, 768, 0, stream>>>(obuf2, ws + OFF_WOB, xmid, xout, nullptr);
  }
}
```

```cpp
#include <hip/hip_runtime.h>
#include <hip/hip_bf16.h>
#include <stdint.h>
#include <type_traits>

typedef __attribute__((ext_vector_type(8))) short bf16x8;
typedef __attribute__((ext_vector_type(4))) float f32x4;
typedef __attribute__((ext_vector_type(2))) float f32x2;
typedef __attribute__((ext_vector_type(16))) float f32x16;
typedef __attribute__((ext_vector_type(4))) unsigned u32x4;
typedef __attribute__((ext_vector_type(4), aligned(4))) unsigned u32x4_a4;
typedef __attribute__((ext_vector_type(2))) unsigned u32x2;
typedef __attribute__((ext_vector_type(2))) __bf16 bf16x2_t;
typedef unsigned short u16;

#define DI __device__ __forceinline__

constexpr int SEQ = 2048;
constexpr int DM = 576;
constexpr int FRAG_TILE = 18 * 1024;

constexpr int REC = 2368;
constexpr int REC_K1 = 1024;
constexpr int REC_V = 1152;
constexpr int KV_BYTES = 64 * REC;
constexpr int CHUNK = 16 * REC;

constexpr size_t OFF_WA = 0;
constexpr size_t OFF_WOA = OFF_WA + 39 * (size_t)FRAG_TILE;
constexpr size_t OFF_WB = OFF_WOA + 36 * (size_t)FRAG_TILE;
constexpr size_t OFF_WOB = OFF_WB + 112 * (size_t)FRAG_TILE;
constexpr size_t ACT_BYTES = (size_t)SEQ * DM * 2;
constexpr size_t OFF_Q = OFF_WOB + 36 * (size_t)FRAG_TILE;
constexpr size_t OFF_KV = OFF_Q + ACT_BYTES;
constexpr size_t OFF_O = OFF_KV + KV_BYTES;
constexpr size_t OFF_X1 = OFF_O + ACT_BYTES;
constexpr size_t OFF_X2 = OFF_X1 + ACT_BYTES;
constexpr size_t OFF_X3 = OFF_X2 + ACT_BYTES;
constexpr int NCA = 39 * 16, NCB = 112 * 16;
constexpr size_t OFF_CSA = OFF_X3 + ACT_BYTES;
constexpr size_t OFF_CSB = OFF_CSA + 18 * (size_t)NCA * 4;
constexpr size_t OFF_ALT = (OFF_CSB + 18 * (size_t)NCB * 4 + 4095) & ~(size_t)4095;

DI float bf_lo(unsigned u) { return __uint_as_float(u << 16); }
DI float bf_hi(unsigned u) { return __uint_as_float(u & 0xffff0000u); }
DI unsigned pack_bf16(float a, float b) {
  f32x2 v = {a, b};
  bf16x2_t r = __builtin_convertvector(v, bf16x2_t);
  return __builtin_bit_cast(unsigned, r);
}
DI float round_bf16(float a) { return bf_lo(pack_bf16(a, 0.f)); }
template <int CTRL>
DI float dpp_f(float v) {
  return __builtin_bit_cast(float, __builtin_amdgcn_mov_dpp(__builtin_bit_cast(int, v), CTRL, 0xf, 0xf, true));
}
DI float red8(float v) {
  v += dpp_f<0xB1>(v);
  v += dpp_f<0x4E>(v);
  v += dpp_f<0x141>(v);
  return v;
}
DI float dot2_bf16(unsigned a, unsigned b, float c) {
  return __builtin_amdgcn_fdot2_f32_bf16(__builtin_bit_cast(bf16x2_t, a), __builtin_bit_cast(bf16x2_t, b), c, false);
}

constexpr int PREP_FAST = 55 * 18;
constexpr int PREP_WAVES = PREP_FAST + 3 * 18;
__global__ __launch_bounds__(256) void prep_kernel(const u16* __restrict__ wq, const u16* __restrict__ wk,
                                                   const u16* __restrict__ wv, const u16* __restrict__ woa,
                                                   const u16* __restrict__ wqkv, const u16* __restrict__ wob,
                                                   char* __restrict__ ws) {
  const int wid = blockIdx.x * 4 + (threadIdx.x >> 6);
  const int lane = threadIdx.x & 63;
  if (wid >= PREP_WAVES) return;
  const int g = lane >> 4, c = lane & 15;
  if (wid < PREP_FAST) {
    const int grp = wid / 18, s = wid % 18;
    const u16* src;
    int ld = 576;
    bool zero = false;
    char* dst;
    float* cs = nullptr;
    if (grp < 9) {
      dst = ws + OFF_WA + (size_t)grp * 4 * FRAG_TILE;
      cs = (float*)(ws + OFF_CSA) + s * NCA + grp * 64 + c * 4;
      src = wq + grp * 64 + c * 4;
    } else if (grp < 18) {
      dst = ws + OFF_WOA + (size_t)(grp - 9) * 4 * FRAG_TILE;
      src = woa + (grp - 9) * 64 + c * 4;
    } else if (grp < 27) {
      dst = ws + OFF_WOB + (size_t)(grp - 18) * 4 * FRAG_TILE;
      src = wob + (grp - 18) * 64 + c * 4;
    } else {
      dst = ws + OFF_WB + (size_t)(grp - 27) * 4 * FRAG_TILE;
      const int np = (grp - 27) * 64 + c * 4;
      cs = (float*)(ws + OFF_CSB) + s * NCB + np;
      const int hg = np / 224, n = np % 224;
      ld = 1728;
      zero = n >= 216;
      const int nn = zero ? 0 : n;
      src = wqkv + (nn / 72) * 576 + hg * 72 + (nn % 72);
    }
    u32x2 r[8];
    const int k0 = 32 * s + 8 * g;
#pragma unroll
    for (int j = 0; j < 8; ++j) r[j] = *(const u32x2*)(src + (size_t)(k0 + j) * ld);
    if (zero) {
#pragma unroll
      for (int j = 0; j < 8; ++j) r[j] = u32x2{0u, 0u};
    }
    char* dp = dst + (size_t)(c >> 2) * FRAG_TILE + s * 1024 + (g * 16 + (c & 3) * 4) * 16;
    f32x4 csum;
#pragma unroll
    for (int q = 0; q < 4; ++q) {
      u32x4 v;
      float a = 0.f;
#pragma unroll
      for (int j = 0; j < 4; ++j) {
        v[j] = __builtin_amdgcn_perm(r[2 * j + 1][q >> 1], r[2 * j][q >> 1], (q & 1) ? 0x07060302u : 0x05040100u);
        a = dot2_bf16(v[j], 0x3f803f80u, a);
      }
      *(u32x4*)(dp + q * 16) = v;
      a += __shfl_xor(a, 16);
      a += __shfl_xor(a, 32);
      csum[q] = a;
    }
    if (cs != nullptr && g == 0) *(f32x4*)cs = csum;
  } else {
    const int w2 = wid - PREP_FAST;
    const int tile = 36 + w2 / 18, s = w2 % 18;
    const int n = tile * 16 + c;
    const u16* src = (n < 594) ? wk + (n - 576) : wv + (n < 612 ? n - 594 : 0);
    const int k0 = 32 * s + 8 * g;
    u32x4 v;
    float a = 0.f;
#pragma unroll
    for (int j = 0; j < 4; ++j) {
      unsigned lo = src[(size_t)(k0 + 2 * j) * 18];
      unsigned hi = src[(size_t)(k0 + 2 * j + 1) * 18];
      v[j] = n < 612 ? (lo | (hi << 16)) : 0u;
      a = dot2_bf16(v[j], 0x3f803f80u, a);
    }
    a += __shfl_xor(a, 16);
    a += __shfl_xor(a, 32);
    if (g == 0) ((float*)(ws + OFF_CSA))[s * NCA + n] = a;
    *(u32x4*)(ws + OFF_WA + (size_t)tile * FRAG_TILE + s * 1024 + lane * 16) = v;
  }
}

template <int MODE>
struct GemmCfg {
  static constexpr int ROWS = (MODE == 2) ? 80 : 64;
  static constexpr int RT = ROWS / 16;
  static constexpr int NT = (MODE == 2) ? 14 : 6;
  static constexpr int KS = (MODE == 2) ? 1 : 2;
  static constexpr int NW = NT * KS;
  static constexpr int KSTEPS = 18 / KS;
  static constexpr int PF = (MODE == 2) ? 5 : 4;
  static constexpr int CB = (MODE == 0) ? 7 : (MODE == 1 ? 6 : 8);
  static constexpr int NACC = RT;
  static constexpr int SPC = KSTEPS / 3;
  static constexpr int NC = (MODE == 2) ? NCB : NCA;
  static constexpr int OFF_STAT = ROWS * 1152;
  static constexpr int OFF_RED = OFF_STAT + ROWS * 8;
  static constexpr int ET = RT / KS;
  static constexpr int SMEM = OFF_RED + (KS == 2 ? NW * ET * 1024 : 0);
};

template <int MODE>
__global__ __launch_bounds__(GemmCfg<MODE>::NW * 64) void gemm_kernel(const u16* __restrict__ A, const char* __restrict__ Wf,
                                                                      const u16* __restrict__ resid, u16* __restrict__ out,
                                                                      char* __restrict__ kvout) {
  using C = GemmCfg<MODE>;
  __shared__ __attribute__((aligned(16))) char smem[C::SMEM];
  const int tid = threadIdx.x, lane = tid & 63, wave = __builtin_amdgcn_readfirstlane(tid >> 6);
  const int xcd = blockIdx.x & 7, idx = blockIdx.x >> 3;
  const int rb = xcd * 4 + idx / C::CB, cb = idx % C::CB;
  int tcol, kh, li;
  bool loader;
  if (MODE == 2) {
    loader = !((wave & 8) && !(wave & 2));
    li = wave < 8 ? wave : wave - 2;
    tcol = loader ? 4 + li : ((wave & 1) | ((wave >> 1) & 2));
    kh = 0;
  } else {
    tcol = wave % C::NT;
    kh = wave / C::NT;
    loader = wave < 8;
    li = wave;
  }
  const int gtile = cb * C::NT + tcol;
  const bool active = (MODE != 0) || (gtile < 39);
  const int row0 = rb * 64 - (MODE == 2 ? 16 : 0);
  const int mloc = lane & 15, g = lane >> 4, nq = g * 4;

  f32x4 acc[C::NACC];
#pragma unroll
  for (int t = 0; t < C::NACC; ++t) acc[t] = f32x4{0.f, 0.f, 0.f, 0.f};
  u32x2 xr[C::ET];
  f32x4 cs0, cs1, colsum;
  const int erow = row0 + (C::KS == 2 ? kh * 32 : 0);

  auto body = [&](auto ldc) {
    constexpr bool LD = decltype(ldc)::value;
    constexpr int T0 = (MODE == 2 && !LD) ? 1 : 0;
    const int sr = li * 8 + (lane >> 3), sj = lane & 7;
    u32x4 xa[9];
    int grow = row0 + sr;
    if (MODE == 2) grow = grow < 0 ? 0 : grow;
    const u16* ap = A + (size_t)grow * DM + sj * 8;
    char* lp = smem + sr * 1152 + ((sj ^ ((sr >> 1) & 7)) * 16);
    if (LD) {
#pragma unroll
      for (int i = 0; i < 3; ++i) xa[i] = *(const u32x4*)(ap + i * 64);
    }
    __builtin_amdgcn_sched_barrier(0);
    __builtin_amdgcn_s_barrier();
    __builtin_amdgcn_sched_barrier(0);
    bf16x8 wf[C::KSTEPS];
    const char* wp = Wf + (size_t)(active ? gtile : 0) * FRAG_TILE + kh * 1024 + lane * 16;
#pragma unroll
    for (int s = 0; s < C::PF; ++s) wf[s] = *(const bf16x8*)(wp + s * C::KS * 1024);
    __builtin_amdgcn_sched_barrier(0);
    if (LD) {
#pragma unroll
      for (int i = 3; i < 9; ++i) xa[i] = *(const u32x4*)(ap + i * 64);
    }
    __builtin_amdgcn_sched_barrier(0);
    float sum = 0.f, sq = 0.f;
    auto stage = [&](int c) {
      __builtin_amdgcn_s_setprio(3);
#pragma unroll
      for (int i = 3 * c; i < 3 * c + 3; ++i) *(u32x4*)(lp + i * 128) = xa[i];
      if (MODE != 1) {
#pragma unroll
        for (int i = 3 * c; i < 3 * c + 3; ++i)
#pragma unroll
          for (int j = 0; j < 4; ++j) {
            sum = dot2_bf16(xa[i][j], 0x3f803f80u, sum);
            sq = dot2_bf16(xa[i][j], xa[i][j], sq);
          }
        if (c == 2) {
          sum = red8(sum);
          sq = red8(sq);
          const float mean = sum * (1.f / 576.f);
          float var = sq * (1.f / 576.f) - mean * mean;
          if (__any(!(mean * mean <= 16.f * var))) {
            var = 0.f;
#pragma unroll
            for (int i = 0; i < 9; ++i)
#pragma unroll
              for (int j = 0; j < 4; ++j) {
                float a = bf_lo(xa[i][j]) - mean, b = bf_hi(xa[i][j]) - mean;
                var += a * a + b * b;
              }
            var = red8(var) * (1.f / 576.f);
          }
          const float rstd = 1.0f / sqrtf(var + 1e-5f);
          if (sj == 0) *(f32x2*)(smem + C::OFF_STAT + sr * 8) = f32x2{mean, rstd};
        }
      }
      __builtin_amdgcn_s_setprio(0);
    };
    if (LD) stage(0);
    __syncthreads();
    __builtin_amdgcn_sched_barrier(0);
    const int be = (g ^ (mloc >> 1)) * 16;
    const char* pA = smem + mloc * 1152 + (kh ? (be ^ 64) + 32 * 1152 : be);
    const char* pB = smem + mloc * 1152 + (C::KS == 2 ? (kh ? (be ^ 64) - 32 * 1152 : be) : (be ^ 64));
#pragma unroll
    for (int s = 0; s < C::KSTEPS; ++s) {
      if (s + C::PF < C::KSTEPS) wf[s + C::PF] = *(const bf16x8*)(wp + (s + C::PF) * C::KS * 1024);
      if (s + C::PF == (MODE == 1 ? C::KSTEPS : C::KSTEPS - 3)) {
        if (MODE == 1) {
#pragma unroll
          for (int t = 0; t < C::ET; ++t) xr[t] = *(const u32x2*)(resid + (size_t)(erow + t * 16 + mloc) * DM + gtile * 16 + nq);
        } else {
          const float* cs = (const float*)resid + (active ? gtile : 0) * 16 + nq;
          cs0 = *(const f32x4*)(cs + mloc * C::NC);
          cs1 = *(const f32x4*)(cs + (16 + (mloc & 1)) * C::NC);
        }
      }
#pragma unroll
      for (int t = T0; t < C::RT; ++t) {
        const char* ap2 = (C::KS == 2) ? (t < 2 ? pA : pB) + s * 128 : ((s & 1) ? pB : pA) + (s >> 1) * 128;
        bf16x8 af = *(const bf16x8*)(ap2 + t * 16 * 1152);
        acc[t] = __builtin_amdgcn_mfma_f32_16x16x32_bf16(wf[s], af, acc[t], 0, 0, 0);
      }
      if (MODE != 1 && s == C::KSTEPS - 2) {
#pragma unroll
        for (int i = 0; i < 4; ++i) {
          float v = cs0[i] + (mloc < 2 ? cs1[i] : 0.f);
          v = red8(v);
          v += dpp_f<0x140>(v);
          colsum[i] = v;
        }
      }
      __builtin_amdgcn_sched_barrier(0);
      if (LD && s % C::SPC == C::SPC - 2 && s / C::SPC < 2) stage(s / C::SPC + 1);
      if (s % C::SPC == C::SPC - 1 && s / C::SPC < 2) __syncthreads();
      __builtin_amdgcn_sched_barrier(0);
    }
  };
  using TT = std::integral_constant<bool, true>;
  using FF = std::integral_constant<bool, false>;
  if (loader) body(TT{});
  else body(FF{});
  if (C::KS == 2) {
    char* red = smem + C::OFF_RED + tcol * 4096 + lane * 16;
#pragma unroll
    for (int t = 0; t < 2; ++t) *(f32x4*)(red + (kh * 2 + t) * 1024) = acc[2 + t];
    __syncthreads();
#pragma unroll
    for (int t = 0; t < 2; ++t) acc[t] += *(const f32x4*)(red + ((kh ^ 1) * 2 + t) * 1024);
  } else {
    __syncthreads();
  }
  if (MODE != 1) {
#pragma unroll
    for (int t = 0; t < C::ET; ++t) {
      const f32x2 st = *(const f32x2*)(smem + C::OFF_STAT + (erow - row0 + t * 16 + mloc) * 8);
#pragma unroll
      for (int i = 0; i < 4; ++i) acc[t][i] = (acc[t][i] - st[0] * colsum[i]) * st[1];
    }
  }

  if (MODE == 1) {
    const int n = gtile * 16 + nq;
#pragma unroll
    for (int t = 0; t < C::ET; ++t) {
      const size_t off = (size_t)(erow + t * 16 + mloc) * DM + n;
      float r0 = round_bf16(acc[t][0]), r1 = round_bf16(acc[t][1]), r2 = round_bf16(acc[t][2]), r3 = round_bf16(acc[t][3]);
      u32x2 o;
      o[0] = pack_bf16(bf_lo(xr[t][0]) + r0, bf_hi(xr[t][0]) + r1);
      o[1] = pack_bf16(bf_lo(xr[t][1]) + r2, bf_hi(xr[t][1]) + r3);
      *(u32x2*)(out + off) = o;
    }
  } else if (MODE == 0) {
    if (!active) return;
    const float qs = 0.23570226039551584f * 1.4426950408889634f;
    if (gtile < 36) {
#pragma unroll
      for (int t = 0; t < C::ET; ++t) {
        const int pos = erow + t * 16 + mloc;
        u32x2 o;
        o[0] = pack_bf16(acc[t][0] * qs, acc[t][1] * qs);
        o[1] = pack_bf16(acc[t][2] * qs, acc[t][3] * qs);
        *(u32x2*)(out + (size_t)pos * DM + gtile * 16 + nq) = o;
      }
    } else {
#pragma unroll
      for (int t = 0; t < C::ET; ++t) {
        const int pos = erow + t * 16 + mloc;
        const int r = pos & 31;
        char* rec = kvout + (pos >> 5) * REC;
        const unsigned w0 = pack_bf16(acc[t][0], acc[t][1]), w1 = pack_bf16(acc[t][2], acc[t][3]);
        if (gtile == 36) {
          *(u32x2*)(rec + (g >> 1) * 512 + r * 16 + (g & 1) * 8) = u32x2{w0, w1};
        } else {
          const int s = r >> 4, rr = r & 15;
          const int hh = (rr >> 2) & 1, j = ((rr >> 3) << 2) | (rr & 3);
          char* vb = rec + REC_V + s * 608 + hh * 304 + j * 2;
          const int d0 = (gtile - 37) * 16 + nq - 2;
          if (d0 < 0) *(unsigned*)(rec + REC_K1 + r * 4) = w0;
          else if (d0 < 18) {
            *(u16*)(vb + d0 * 16) = (u16)(w0 & 0xffffu);
            *(u16*)(vb + (d0 + 1) * 16) = (u16)(w0 >> 16);
          } else if (d0 == 18) *(u16*)(vb + 18 * 16) = (u16)0x3f80;
          if (d0 + 2 < 18) {
            *(u16*)(vb + (d0 + 2) * 16) = (u16)(w1 & 0xffffu);
            *(u16*)(vb + (d0 + 3) * 16) = (u16)(w1 >> 16);
          }
        }
      }
    }
  } else {
    constexpr int LDQ = 456;
#pragma unroll
    for (int t = 0; t < C::RT; ++t) {
      u32x2 o;
      o[0] = pack_bf16(acc[t][0], acc[t][1]);
      o[1] = pack_bf16(acc[t][2], acc[t][3]);
      *(u32x2*)(smem + (t * 16 + mloc) * LDQ + (tcol * 16 + nq) * 2) = o;
    }
    __syncthreads();
    if (tid < 512) {
      const int half = tid & 1, hh = ((tid >> 1) & 1) | ((tid >> 4) & 2), i = ((tid >> 2) & 7) | ((tid >> 6) << 3);
      const int pos = rb * 64 + i;
      const char* qrow = smem + (16 + i) * LDQ + hh * 36;
      unsigned q[9];
#pragma unroll
      for (int d = 0; d < 9; ++d) q[d] = *(const unsigned*)(qrow + d * 4);
      const char* kbase = qrow - 8 * half * LDQ;
      float lg[4];
      float mx = -3.0e38f;
#pragma unroll
      for (int w = 0; w < 4; ++w) {
        const char* krow = kbase - 2 * w * LDQ + 144;
        float sacc = 0.f;
#pragma unroll
        for (int d = 0; d < 9; ++d) sacc = dot2_bf16(q[d], *(const unsigned*)(krow + d * 4), sacc);
        sacc *= 0.23570226039551584f;
        lg[w] = (pos - 2 * (w + 4 * half) >= 0) ? sacc : -1e9f;
        mx = fmaxf(mx, lg[w]);
      }
      mx = fmaxf(mx, dpp_f<0xB1>(mx));
      float den = 0.f;
#pragma unroll
      for (int w = 0; w < 4; ++w) {
        lg[w] = __expf(lg[w] - mx);
        den += lg[w];
      }
      den += dpp_f<0xB1>(den);
      const float inv = 1.f / den;
      float o[18];
#pragma unroll
      for (int d = 0; d < 18; ++d) o[d] = 0.f;
#pragma unroll
      for (int w = 0; w < 4; ++w) {
        const char* vrow = kbase - 2 * w * LDQ + 288;
        const float p = lg[w] * inv;
#pragma unroll
        for (int d = 0; d < 9; ++d) {
          unsigned u = *(const unsigned*)(vrow + d * 4);
          o[2 * d] += p * bf_lo(u);
          o[2 * d + 1] += p * bf_hi(u);
        }
      }
#pragma unroll
      for (int d = 0; d < 18; ++d) o[d] += dpp_f<0xB1>(o[d]);
      unsigned* op = (unsigned*)(out + (size_t)pos * DM + (cb * 4 + hh) * 18);
      if (half == 0) {
#pragma unroll
        for (int d = 0; d < 5; ++d) op[d] = pack_bf16(o[2 * d], o[2 * d + 1]);
      } else {
#pragma unroll
        for (int d = 5; d < 9; ++d) op[d] = pack_bf16(o[2 * d], o[2 * d + 1]);
      }
    }
  }
}

#define MFMA32(a, b, c) __builtin_amdgcn_mfma_f32_32x32x16_bf16(a, b, c, 0, 0, 0)
#define MFMA8(a, b, c) __builtin_amdgcn_mfma_f32_32x32x8bf16_1k(__builtin_bit_cast(bf16x4, a), __builtin_bit_cast(bf16x4, b), c, 0, 0, 0)
#define EXP2(x) __builtin_amdgcn_exp2f(x)
typedef __attribute__((ext_vector_type(4))) short bf16x4;

constexpr int CH2 = 8 * REC;
__global__ __launch_bounds__(1024) void attn_kernel(const u16* __restrict__ qin, const char* __restrict__ kvin,
                                                    u16* __restrict__ out) {
  __shared__ __attribute__((aligned(16))) char smem[66 * REC];
  const int tid = threadIdx.x, lane = tid & 63, wave = __builtin_amdgcn_readfirstlane(tid >> 6);
  const int pw = wave & 7, half = wave >> 3;
  const int r = lane & 31, h = lane >> 5;
  const int hb = half * 32 * REC;
  u32x4 st[3];
  int soff[3];
#pragma unroll
  for (int j = 0; j < 3; ++j) {
    const int o = (pw + 8 * j) * 1024 + lane * 16;
    soff[j] = hb + (o < CH2 ? o : lane * 16);
  }
#pragma unroll
  for (int j = 0; j < 3; ++j) st[j] = *(const u32x4*)(kvin + soff[j]);
  const int pos = blockIdx.x * 8 + pw;
  const u16* qp = qin + (size_t)pos * DM + r * 18;
  u32x4 q0u = *(const u32x4_a4*)(qp + h * 8);
  unsigned q1w = *(const unsigned*)(qp + 16);
  if (h) q1w = 0u;
  const bf16x8 q0 = __builtin_bit_cast(bf16x8, q0u);
  const u32x2 q1f = {q1w, 0u};
#pragma unroll
  for (int j = 0; j < 3; ++j) *(u32x4*)(smem + soff[j]) = st[j];
  __syncthreads();
#pragma unroll
  for (int j = 0; j < 3; ++j) st[j] = *(const u32x4*)(kvin + CH2 + soff[j]);
  __builtin_amdgcn_sched_barrier(0);

  const char* k0p = smem + hb + h * 512 + r * 16;
  const char* k1p = smem + hb + (h ? REC_V + 18 * 16 : REC_K1 + r * 4);
  const char* vp = smem + hb + REC_V + h * 304 + (r < 18 ? r : 18) * 16;

  f32x16 zero;
#pragma unroll
  for (int i = 0; i < 16; ++i) zero[i] = 0.f;
  f32x16 acc = zero;
  f32x16 sA, sB;
  float m;
  u32x2 q1;
  {
    bf16x8 k0 = *(const bf16x8*)(k0p);
    u32x2 k1 = {*(const unsigned*)(k1p), 0u};
    f32x16 s = MFMA32(k0, q0, zero);
    s = MFMA8(k1, q1f, s);
    float mx = s[0];
#pragma unroll
    for (int i = 1; i < 16; ++i) mx = fmaxf(mx, s[i]);
    mx = fmaxf(mx, __shfl_xor(mx, 32));
    const float mhi = round_bf16(mx), mlo = round_bf16(mx - mhi);
    m = mhi + mlo;
    q1 = u32x2{h ? pack_bf16(-mhi, -mlo) : q1w, 0u};
#pragma unroll
    for (int i = 0; i < 16; ++i) sA[i] = s[i] - m;
  }
  bf16x8 k0A = *(const bf16x8*)(k0p + REC), k0B;
  u32x2 k1A = {*(const unsigned*)(k1p + REC), 0u}, k1B = {0u, 0u};
  bf16x8 v0A = {0, 0, 0, 0, 0, 0, 0, 0}, v1A = v0A, v0B, v1B;
  u32x4 pA0 = {0u, 0u, 0u, 0u}, pA1 = pA0, pB0, pB1;

#define SB __builtin_amdgcn_sched_barrier(0);
#define TILE_BODY(OFF, SC, SN, K0C, K1C, V0C, V1C, K0N, K1N, V0N, V1N, PP0, PP1, PC0, PC1)                     \
  {                                                                                                            \
    acc = MFMA32(V0C, __builtin_bit_cast(bf16x8, PP0), acc);                                                   \
    K0N = *(const bf16x8*)(k0p + (OFF) + 2 * REC);                                                             \
    K1N[0] = *(const unsigned*)(k1p + (OFF) + 2 * REC);                                                        \
    SB                                                                                                         \
    PC0[0] = pack_bf16(EXP2(SC[0]), EXP2(SC[1]));                                                              \
    PC0[1] = pack_bf16(EXP2(SC[2]), EXP2(SC[3]));                                                              \
    SB                                                                                                         \
    acc = MFMA32(V1C, __builtin_bit_cast(bf16x8, PP1), acc);                                                   \
    V0N = *(const bf16x8*)(vp + (OFF));                                                                        \
    V1N = *(const bf16x8*)(vp + (OFF) + 608);                                                                  \
    SB                                                                                                         \
    PC0[2] = pack_bf16(EXP2(SC[4]), EXP2(SC[5]));                                                              \
    PC0[3] = pack_bf16(EXP2(SC[6]), EXP2(SC[7]));                                                              \
    SB                                                                                                         \
    SN = MFMA32(K0C, q0, zero);                                                                                \
    SB                                                                                                         \
    PC1[0] = pack_bf16(EXP2(SC[8]), EXP2(SC[9]));                                                              \
    PC1[1] = pack_bf16(EXP2(SC[10]), EXP2(SC[11]));                                                            \
    SB                                                                                                         \
    SN = MFMA8(K1C, q1, SN);                                                                                   \
    SB                                                                                                         \
    PC1[2] = pack_bf16(EXP2(SC[12]), EXP2(SC[13]));                                                            \
    PC1[3] = pack_bf16(EXP2(SC[14]), EXP2(SC[15]));                                                            \
    SB                                                                                                         \
  }

  for (int t = 0; t < 32; t += 4) {
    if ((t & 7) == 4 && t < 24) {
      const int c = (t >> 3) + 1;
#pragma unroll
      for (int j = 0; j < 3; ++j) *(u32x4*)(smem + c * CH2 + soff[j]) = st[j];
      if (c < 3) {
#pragma unroll
        for (int j = 0; j < 3; ++j) st[j] = *(const u32x4*)(kvin + (c + 1) * CH2 + soff[j]);
      }
      asm volatile("s_waitcnt lgkmcnt(0)" ::: "memory");
      __builtin_amdgcn_s_barrier();
      asm volatile("" ::: "memory");
    }
    const int off = t * REC;
    TILE_BODY(off, sA, sB, k0A, k1A, v0A, v1A, k0B, k1B, v0B, v1B, pA0, pA1, pB0, pB1)
    TILE_BODY(off + REC, sB, sA, k0B, k1B, v0B, v1B, k0A, k1A, v0A, v1A, pB0, pB1, pA0, pA1)
    TILE_BODY(off + 2 * REC, sA, sB, k0A, k1A, v0A, v1A, k0B, k1B, v0B, v1B, pA0, pA1, pB0, pB1)
    TILE_BODY(off + 3 * REC, sB, sA, k0B, k1B, v0B, v1B, k0A, k1A, v0A, v1A, pB0, pB1, pA0, pA1)
  }
  acc = MFMA32(v0A, __builtin_bit_cast(bf16x8, pA0), acc);
  acc = MFMA32(v1A, __builtin_bit_cast(bf16x8, pA1), acc);

  {
    bool bad = !(acc[10] < 1.0e30f);
#pragma unroll
    for (int i = 0; i < 10; ++i) bad = bad || !(fabsf(acc[i]) < 3.0e38f);
    if (__any(bad)) {
      acc = zero;
      m = -3.0e38f;
      for (int t = 0; t < 32; ++t) {
        bf16x8 k0 = *(const bf16x8*)(k0p + t * REC);
        u32x2 k1 = {*(const unsigned*)(k1p + t * REC), 0u};
        bf16x8 v0 = *(const bf16x8*)(vp + t * REC);
        bf16x8 v1 = *(const bf16x8*)(vp + t * REC + 608);
        f32x16 s = MFMA32(k0, q0, zero);
        s = MFMA8(k1, q1f, s);
        float mx = s[0];
#pragma unroll
        for (int i = 1; i < 16; ++i) mx = fmaxf(mx, s[i]);
        mx = fmaxf(mx, __shfl_xor(mx, 32));
        const float mnew = fmaxf(m, mx);
        const float sc = EXP2(m - mnew);
        m = mnew;
#pragma unroll
        for (int i = 0; i < 16; ++i) acc[i] *= sc;
        u32x4 p0, p1;
#pragma unroll
        for (int i = 0; i < 4; ++i) {
          p0[i] = pack_bf16(EXP2(s[2 * i] - m), EXP2(s[2 * i + 1] - m));
          p1[i] = pack_bf16(EXP2(s[8 + 2 * i] - m), EXP2(s[8 + 2 * i + 1] - m));
        }
        acc = MFMA32(v0, __builtin_bit_cast(bf16x8, p0), acc);
        acc = MFMA32(v1, __builtin_bit_cast(bf16x8, p1), acc);
      }
    }
  }
  __syncthreads();
  float* xp = (float*)(smem + pw * 3072) + lane;
  if (half) {
#pragma unroll
    for (int i = 0; i < 11; ++i) xp[i * 64] = acc[i];
    xp[11 * 64] = m;
  }
  __syncthreads();
  if (half) return;
  {
    const float m1 = xp[11 * 64];
    const float mm = fmaxf(m, m1);
    const float a0 = EXP2(m - mm), a1 = EXP2(m1 - mm);
#pragma unroll
    for (int i = 0; i < 11; ++i) acc[i] = acc[i] * a0 + xp[i * 64] * a1;
  }
  const float inv = 1.f / acc[10];
  u16* op = out + (size_t)pos * DM + r * 18;
  {
    unsigned a0 = pack_bf16(acc[0] * inv, acc[1] * inv), a1 = pack_bf16(acc[2] * inv, acc[3] * inv);
    unsigned b0 = pack_bf16(acc[4] * inv, acc[5] * inv), b1 = pack_bf16(acc[6] * inv, acc[7] * inv);
    unsigned* o32 = (unsigned*)(op + h * 4);
    o32[0] = a0;
    o32[1] = a1;
    o32[4] = b0;
    o32[5] = b1;
    if (!h) *(unsigned*)(op + 16) = pack_bf16(acc[8] * inv, acc[9] * inv);
  }
}

extern "C" void kernel_launch(void* const* d_in, const int* in_sizes, int n_in, void* d_out, int out_size, void* d_ws,
                              size_t ws_size, hipStream_t stream) {
  const u16* x0 = (const u16*)d_in[0];
  const u16* wq = (const u16*)d_in[1];
  const u16* wk = (const u16*)d_in[2];
  const u16* wv = (const u16*)d_in[3];
  const u16* woa = (const u16*)d_in[4];
  const u16* wqkv = (const u16*)d_in[6];
  const u16* wob = (const u16*)d_in[7];
  char* ws = (char*)d_ws;
  u16* x1 = (u16*)(ws + OFF_X1);
  u16* x2 = (u16*)(ws + OFF_X2);
  u16* x3 = (u16*)(ws + OFF_X3);

  prep_kernel<<<(PREP_WAVES + 3) / 4, 256, 0, stream>>>(wq, wk, wv, woa, wqkv, wob, ws);
  for (int layer = 0; layer < 2; ++layer) {
    const u16* xin = layer == 0 ? x0 : x2;
    u16* xmid = layer == 0 ? x1 : x3;
    u16* xout = layer == 0 ? x2 : (u16*)d_out;
    u16* qb = (u16*)(ws + (layer == 0 ? OFF_Q : OFF_ALT));
    char* kvb = ws + (layer == 0 ? OFF_KV : OFF_ALT + ACT_BYTES);
    u16* obuf = (u16*)(ws + (layer == 0 ? OFF_O : OFF_ALT + 2 * ACT_BYTES));
    u16* obuf2 = (u16*)(ws + OFF_ALT + (3 + layer) * ACT_BYTES);
    gemm_kernel<0><<<224, 768, 0, stream>>>(xin, ws + OFF_WA, (const u16*)(ws + OFF_CSA), qb, kvb);
    attn_kernel<<<256, 1024, 0, stream>>>(qb, kvb, obuf);
    gemm_kernel<1><<<192, 768, 0, stream>>>(obuf, ws + OFF_WOA, xin, xmid, nullptr);
    gemm_kernel<2><<<256, 896, 0, stream>>>(xmid, ws + OFF_WB, (const u16*)(ws + OFF_CSB), obuf2, nullptr);
    gemm_kernel<1><<<192, 768, 0, stream>>>(obuf2, ws + OFF_WOB, xmid, xout, nullptr);
  }
}
```

```cpp
#include <hip/hip_runtime.h>
#include <hip/hip_bf16.h>
#include <stdint.h>
#include <type_traits>

typedef __attribute__((ext_vector_type(8))) short bf16x8;
typedef __attribute__((ext_vector_type(4))) float f32x4;
typedef __attribute__((ext_vector_type(2))) float f32x2;
typedef __attribute__((ext_vector_type(16))) float f32x16;
typedef __attribute__((ext_vector_type(4))) unsigned u32x4;
typedef __attribute__((ext_vector_type(4), aligned(4))) unsigned u32x4_a4;
typedef __attribute__((ext_vector_type(2))) unsigned u32x2;
typedef __attribute__((ext_vector_type(2))) __bf16 bf16x2_t;
typedef unsigned short u16;

#define DI __device__ __forceinline__

constexpr int SEQ = 2048;
constexpr int DM = 576;
constexpr int FRAG_TILE = 18 * 1024;

constexpr int REC = 2368;
constexpr int REC_K1 = 1024;
constexpr int REC_V = 1152;
constexpr int KV_BYTES = 64 * REC;
constexpr int CHUNK = 16 * REC;

constexpr size_t OFF_WA = 0;
constexpr size_t OFF_WOA = OFF_WA + 39 * (size_t)FRAG_TILE;
constexpr size_t OFF_WB = OFF_WOA + 36 * (size_t)FRAG_TILE;
constexpr size_t OFF_WOB = OFF_WB + 112 * (size_t)FRAG_TILE;
constexpr size_t ACT_BYTES = (size_t)SEQ * DM * 2;
constexpr size_t OFF_Q = OFF_WOB + 36 * (size_t)FRAG_TILE;
constexpr size_t OFF_KV = OFF_Q + ACT_BYTES;
constexpr size_t OFF_O = OFF_KV + KV_BYTES;
constexpr size_t OFF_X1 = OFF_O + ACT_BYTES;
constexpr size_t OFF_X2 = OFF_X1 + ACT_BYTES;
constexpr size_t OFF_X3 = OFF_X2 + ACT_BYTES;
constexpr int NCA = 39 * 16, NCB = 112 * 16;
constexpr size_t OFF_CSA = OFF_X3 + ACT_BYTES;
constexpr size_t OFF_CSB = OFF_CSA + 18 * (size_t)NCA * 4;
constexpr size_t OFF_ALT = (OFF_CSB + 18 * (size_t)NCB * 4 + 4095) & ~(size_t)4095;

DI float bf_lo(unsigned u) { return __uint_as_float(u << 16); }
DI float bf_hi(unsigned u) { return __uint_as_float(u & 0xffff0000u); }
DI unsigned pack_bf16(float a, float b) {
  f32x2 v = {a, b};
  bf16x2_t r = __builtin_convertvector(v, bf16x2_t);
  return __builtin_bit_cast(unsigned, r);
}
DI float round_bf16(float a) { return bf_lo(pack_bf16(a, 0.f)); }
template <int CTRL>
DI float dpp_f(float v) {
  return __builtin_bit_cast(float, __builtin_amdgcn_mov_dpp(__builtin_bit_cast(int, v), CTRL, 0xf, 0xf, true));
}
DI float red8(float v) {
  v += dpp_f<0xB1>(v);
  v += dpp_f<0x4E>(v);
  v += dpp_f<0x141>(v);
  return v;
}
typedef __attribute__((ext_vector_type(4))) short s16x4_t;
DI u32x2 tr_read(const char* p) {
  return __builtin_bit_cast(u32x2, __builtin_amdgcn_ds_read_tr16_b64_v4i16((__attribute__((address_space(3))) s16x4_t*)p));
}
DI float dot2_bf16(unsigned a, unsigned b, float c) {
  return __builtin_amdgcn_fdot2_f32_bf16(__builtin_bit_cast(bf16x2_t, a), __builtin_bit_cast(bf16x2_t, b), c, false);
}

constexpr int PREP_FAST = 55 * 18;
constexpr int PREP_WAVES = PREP_FAST + 3 * 18;
__global__ __launch_bounds__(256) void prep_kernel(const u16* __restrict__ wq, const u16* __restrict__ wk,
                                                   const u16* __restrict__ wv, const u16* __restrict__ woa,
                                                   const u16* __restrict__ wqkv, const u16* __restrict__ wob,
                                                   char* __restrict__ ws) {
  const int wid = blockIdx.x * 4 + (threadIdx.x >> 6);
  const int lane = threadIdx.x & 63;
  if (wid >= PREP_WAVES) return;
  const int g = lane >> 4, c = lane & 15;
  if (wid < PREP_FAST) {
    const int grp = wid / 18, s = wid % 18;
    const u16* src;
    int ld = 576;
    bool zero = false;
    char* dst;
    float* cs = nullptr;
    if (grp < 9) {
      dst = ws + OFF_WA + (size_t)grp * 4 * FRAG_TILE;
      cs = (float*)(ws + OFF_CSA) + s * NCA + grp * 64 + c * 4;
      src = wq + grp * 64 + c * 4;
    } else if (grp < 18) {
      dst = ws + OFF_WOA + (size_t)(grp - 9) * 4 * FRAG_TILE;
      src = woa + (grp - 9) * 64 + c * 4;
    } else if (grp < 27) {
      dst = ws + OFF_WOB + (size_t)(grp - 18) * 4 * FRAG_TILE;
      src = wob + (grp - 18) * 64 + c * 4;
    } else {
      dst = ws + OFF_WB + (size_t)(grp - 27) * 4 * FRAG_TILE;
      const int np = (grp - 27) * 64 + c * 4;
      cs = (float*)(ws + OFF_CSB) + s * NCB + np;
      const int hg = np / 224, n = np % 224;
      ld = 1728;
      zero = n >= 216;
      const int nn = zero ? 0 : n;
      src = wqkv + (nn / 72) * 576 + hg * 72 + (nn % 72);
    }
    u32x2 r[8];
    const int k0 = 32 * s + 8 * g;
#pragma unroll
    for (int j = 0; j < 8; ++j) r[j] = *(const u32x2*)(src + (size_t)(k0 + j) * ld);
    if (zero) {
#pragma unroll
      for (int j = 0; j < 8; ++j) r[j] = u32x2{0u, 0u};
    }
    char* dp = dst + (size_t)(c >> 2) * FRAG_TILE + s * 1024 + (g * 16 + (c & 3) * 4) * 16;
    f32x4 csum;
#pragma unroll
    for (int q = 0; q < 4; ++q) {
      u32x4 v;
      float a = 0.f;
#pragma unroll
      for (int j = 0; j < 4; ++j) {
        v[j] = __builtin_amdgcn_perm(r[2 * j + 1][q >> 1], r[2 * j][q >> 1], (q & 1) ? 0x07060302u : 0x05040100u);
        a = dot2_bf16(v[j], 0x3f803f80u, a);
      }
      *(u32x4*)(dp + q * 16) = v;
      a += __shfl_xor(a, 16);
      a += __shfl_xor(a, 32);
      csum[q] = a;
    }
    if (cs != nullptr && g == 0) *(f32x4*)cs = csum;
  } else {
    const int w2 = wid - PREP_FAST;
    const int tile = 36 + w2 / 18, s = w2 % 18;
    const int n = tile * 16 + c;
    const u16* src = (n < 594) ? wk + (n - 576) : wv + (n < 612 ? n - 594 : 0);
    const int k0 = 32 * s + 8 * g;
    u32x4 v;
    float a = 0.f;
#pragma unroll
    for (int j = 0; j < 4; ++j) {
      unsigned lo = src[(size_t)(k0 + 2 * j) * 18];
      unsigned hi = src[(size_t)(k0 + 2 * j + 1) * 18];
      v[j] = n < 612 ? (lo | (hi << 16)) : 0u;
      a = dot2_bf16(v[j], 0x3f803f80u, a);
    }
    a += __shfl_xor(a, 16);
    a += __shfl_xor(a, 32);
    if (g == 0) ((float*)(ws + OFF_CSA))[s * NCA + n] = a;
    *(u32x4*)(ws + OFF_WA + (size_t)tile * FRAG_TILE + s * 1024 + lane * 16) = v;
  }
}

template <int MODE>
struct GemmCfg {
  static constexpr int ROWS = (MODE == 2) ? 80 : 64;
  static constexpr int RT = ROWS / 16;
  static constexpr int NT = (MODE == 2) ? 14 : 5;
  static constexpr int KS = (MODE == 2) ? 1 : 2;
  static constexpr int NW = NT * KS;
  static constexpr int KSTEPS = 18 / KS;
  static constexpr int PF = (MODE == 2) ? 5 : 4;
  static constexpr int CB = 8;
  static constexpr int NACC = RT;
  static constexpr int SPC = KSTEPS / 3;
  static constexpr int NC = (MODE == 2) ? NCB : NCA;
  static constexpr int OFF_STAT = ROWS * 1152;
  static constexpr int OFF_RED = OFF_STAT + ROWS * 8;
  static constexpr int ET = RT / KS;
  static constexpr int SMEM = OFF_RED + (KS == 2 ? NW * ET * 1024 : 0);
};

template <int MODE>
__global__ __launch_bounds__(GemmCfg<MODE>::NW * 64) void gemm_kernel(const u16* __restrict__ A, const char* __restrict__ Wf,
                                                                      const u16* __restrict__ resid, u16* __restrict__ out,
                                                                      char* __restrict__ kvout) {
  using C = GemmCfg<MODE>;
  __shared__ __attribute__((aligned(16))) char smem[C::SMEM];
  const int tid = threadIdx.x, lane = tid & 63, wave = __builtin_amdgcn_readfirstlane(tid >> 6);
  const int xcd = blockIdx.x & 7, idx = blockIdx.x >> 3;
  const int rb = xcd * 4 + idx / C::CB, cb = idx % C::CB;
  int tcol, kh, li;
  bool loader;
  if (MODE == 2) {
    loader = !((wave & 8) && !(wave & 2));
    li = wave < 8 ? wave : wave - 2;
    tcol = loader ? 4 + li : ((wave & 1) | ((wave >> 1) & 2));
    kh = 0;
  } else {
    tcol = wave % C::NT;
    kh = wave / C::NT;
    loader = wave < 8;
    li = wave;
  }
  const int gtile = cb * C::NT + tcol;
  const bool active = (MODE == 2) || (gtile < (MODE == 0 ? 39 : 36));
  const int row0 = rb * 64 - (MODE == 2 ? 16 : 0);
  const int mloc = lane & 15, g = lane >> 4, nq = g * 4;

  f32x4 acc[C::NACC];
#pragma unroll
  for (int t = 0; t < C::NACC; ++t) acc[t] = f32x4{0.f, 0.f, 0.f, 0.f};
  u32x2 xr[C::ET];
  f32x4 cs0, cs1, colsum;
  constexpr int LDQ = 456;
  f32x4 bias0, bias1;
  int oq0, oq1, ok0, ok1, ov0, ov1, oo;
  unsigned pm;
  const int erow = row0 + (C::KS == 2 ? kh * 32 : 0);

  auto body = [&](auto ldc) {
    constexpr bool LD = decltype(ldc)::value;
    constexpr int T0 = (MODE == 2 && !LD) ? 1 : 0;
    const int sr = li * 8 + (lane >> 3), sj = lane & 7;
    u32x4 xa[9];
    int grow = row0 + sr;
    if (MODE == 2) grow = grow < 0 ? 0 : grow;
    const u16* ap = A + (size_t)grow * DM + sj * 8;
    char* lp = smem + sr * 1152 + ((sj ^ ((sr >> 1) & 7)) * 16);
    if (LD) {
#pragma unroll
      for (int i = 0; i < 3; ++i) xa[i] = *(const u32x4*)(ap + i * 64);
    }
    __builtin_amdgcn_sched_barrier(0);
    __builtin_amdgcn_s_barrier();
    __builtin_amdgcn_sched_barrier(0);
    bf16x8 wf[C::KSTEPS];
    const char* wp = Wf + (size_t)(active ? gtile : cb * C::NT) * FRAG_TILE + kh * 1024 + lane * 16;
#pragma unroll
    for (int s = 0; s < C::PF; ++s) wf[s] = *(const bf16x8*)(wp + s * C::KS * 1024);
    __builtin_amdgcn_sched_barrier(0);
    if (LD) {
#pragma unroll
      for (int i = 3; i < 9; ++i) xa[i] = *(const u32x4*)(ap + i * 64);
    }
    __builtin_amdgcn_sched_barrier(0);
    float sum = 0.f, sq = 0.f;
    auto stage = [&](int c) {
      __builtin_amdgcn_s_setprio(3);
#pragma unroll
      for (int i = 3 * c; i < 3 * c + 3; ++i) *(u32x4*)(lp + i * 128) = xa[i];
      if (MODE != 1) {
#pragma unroll
        for (int i = 3 * c; i < 3 * c + 3; ++i)
#pragma unroll
          for (int j = 0; j < 4; ++j) {
            sum = dot2_bf16(xa[i][j], 0x3f803f80u, sum);
            sq = dot2_bf16(xa[i][j], xa[i][j], sq);
          }
        if (c == 2) {
          sum = red8(sum);
          sq = red8(sq);
          const float mean = sum * (1.f / 576.f);
          float var = sq * (1.f / 576.f) - mean * mean;
          if (__any(!(mean * mean <= 16.f * var))) {
            var = 0.f;
#pragma unroll
            for (int i = 0; i < 9; ++i)
#pragma unroll
              for (int j = 0; j < 4; ++j) {
                float a = bf_lo(xa[i][j]) - mean, b = bf_hi(xa[i][j]) - mean;
                var += a * a + b * b;
              }
            var = red8(var) * (1.f / 576.f);
          }
          const float rstd = 1.0f / sqrtf(var + 1e-5f);
          if (sj == 0) *(f32x2*)(smem + C::OFF_STAT + sr * 8) = f32x2{mean, rstd};
        }
      }
      __builtin_amdgcn_s_setprio(0);
    };
    if (LD) stage(0);
    __syncthreads();
    __builtin_amdgcn_sched_barrier(0);
    const int be = (g ^ (mloc >> 1)) * 16;
    const char* pA = smem + mloc * 1152 + (kh ? (be ^ 64) + 32 * 1152 : be);
    const char* pB = smem + mloc * 1152 + (C::KS == 2 ? (kh ? (be ^ 64) - 32 * 1152 : be) : (be ^ 64));
#pragma unroll
    for (int s = 0; s < C::KSTEPS; ++s) {
      if (s + C::PF < C::KSTEPS) wf[s + C::PF] = *(const bf16x8*)(wp + (s + C::PF) * C::KS * 1024);
      if (s + C::PF == (MODE == 1 ? C::KSTEPS : C::KSTEPS - 3)) {
        if (MODE == 1) {
#pragma unroll
          for (int t = 0; t < C::ET; ++t) xr[t] = *(const u32x2*)(resid + (size_t)(erow + t * 16 + mloc) * DM + (active ? gtile : cb * C::NT) * 16 + nq);
        } else {
          const float* cs = (const float*)resid + (active ? gtile : cb * C::NT) * 16 + nq;
          cs0 = *(const f32x4*)(cs + mloc * C::NC);
          cs1 = *(const f32x4*)(cs + (16 + (mloc & 1)) * C::NC);
        }
      }
#pragma unroll
      for (int t = T0; t < C::RT; ++t) {
        const char* ap2 = (C::KS == 2) ? (t < 2 ? pA : pB) + s * 128 : ((s & 1) ? pB : pA) + (s >> 1) * 128;
        bf16x8 af = *(const bf16x8*)(ap2 + t * 16 * 1152);
        acc[t] = __builtin_amdgcn_mfma_f32_16x16x32_bf16(wf[s], af, acc[t], 0, 0, 0);
      }
      if (MODE != 1 && s == C::KSTEPS - 2) {
#pragma unroll
        for (int i = 0; i < 4; ++i) {
          float v = cs0[i] + (mloc < 2 ? cs1[i] : 0.f);
          v = red8(v);
          v += dpp_f<0x140>(v);
          colsum[i] = v;
        }
      }
      if (MODE == 2 && s == C::KSTEPS - 3) {
        int ln = lane;
        asm volatile("" : "+v"(ln));
        const int mloc = ln & 15, g = ln >> 4, nq = g * 4;
        const unsigned nb = ~(0x5555u << (mloc + 2)) >> nq;
#pragma unroll
        for (int i = 0; i < 4; ++i) {
          bias0[i] = __builtin_bit_cast(float, (unsigned)((int)(nb << (31 - i)) >> 31) & 0xff800000u);
          bias1[i] = __builtin_bit_cast(float, (unsigned)((int)(nb << (15 - i)) >> 31) & 0xff800000u);
        }
        const int ub = (wave >> 2) * 16 * LDQ, hb = (wave & 3) * 36;
        oq0 = ub + (16 + mloc) * LDQ + (g == 3 ? 432 : hb + g * 16);
        oq1 = ub + (16 + mloc) * LDQ + (g >= 2 ? 432 : hb + g * 16);
        ok0 = ub + mloc * LDQ + 144 + hb + g * 16;
        ok1 = ok0 + 16 * LDQ;
        const bool first = rb == 0 && wave < 4;
        ok0 = first ? mloc * LDQ + 432 : ok0;
        bias0 += first ? -1e9f / 0.23570226039551584f : 0.f;
        const int ha = hb - (wave & 1) * 4;
        ov0 = ub + (nq + (mloc >> 2)) * LDQ + 288 + ha + (mloc & 3) * 8;
        ov1 = ub + (nq + (mloc >> 2)) * LDQ + ((mloc & 3) ? 448 : 320 + ha);
        oo = (rb * 64 + (wave >> 2) * 16 + mloc) * (DM * 2) + cb * 144 + ha + g * 8;
        pm = (mloc & 1) ? 0xffff0000u : 0x0000ffffu;
        asm volatile("" : "+v"(bias0), "+v"(bias1), "+v"(oq0), "+v"(oq1), "+v"(ok0), "+v"(ok1), "+v"(ov0), "+v"(ov1), "+v"(oo), "+v"(pm));
      }
      __builtin_amdgcn_sched_barrier(0);
      if (LD && s % C::SPC == C::SPC - 2 && s / C::SPC < 2) stage(s / C::SPC + 1);
      if (s % C::SPC == C::SPC - 1 && s / C::SPC < 2) __syncthreads();
      __builtin_amdgcn_sched_barrier(0);
    }
  };
  using TT = std::integral_constant<bool, true>;
  using FF = std::integral_constant<bool, false>;
  if (loader) body(TT{});
  else body(FF{});
  if (C::KS == 2) {
    char* red = smem + C::OFF_RED + tcol * 4096 + lane * 16;
#pragma unroll
    for (int t = 0; t < 2; ++t) *(f32x4*)(red + (kh * 2 + t) * 1024) = acc[2 + t];
    __syncthreads();
#pragma unroll
    for (int t = 0; t < 2; ++t) acc[t] += *(const f32x4*)(red + ((kh ^ 1) * 2 + t) * 1024);
  } else {
    __syncthreads();
  }
  if (MODE != 1) {
#pragma unroll
    for (int t = 0; t < C::ET; ++t) {
      const f32x2 st = *(const f32x2*)(smem + C::OFF_STAT + (erow - row0 + t * 16 + mloc) * 8);
#pragma unroll
      for (int i = 0; i < 4; ++i) acc[t][i] = (acc[t][i] - st[0] * colsum[i]) * st[1];
    }
  }

  if (MODE == 1) {
    if (!active) return;
    const int n = gtile * 16 + nq;
#pragma unroll
    for (int t = 0; t < C::ET; ++t) {
      const size_t off = (size_t)(erow + t * 16 + mloc) * DM + n;
      float r0 = round_bf16(acc[t][0]), r1 = round_bf16(acc[t][1]), r2 = round_bf16(acc[t][2]), r3 = round_bf16(acc[t][3]);
      u32x2 o;
      o[0] = pack_bf16(bf_lo(xr[t][0]) + r0, bf_hi(xr[t][0]) + r1);
      o[1] = pack_bf16(bf_lo(xr[t][1]) + r2, bf_hi(xr[t][1]) + r3);
      *(u32x2*)(out + off) = o;
    }
  } else if (MODE == 0) {
    if (!active) return;
    const float qs = 0.23570226039551584f * 1.4426950408889634f;
    if (gtile < 36) {
#pragma unroll
      for (int t = 0; t < C::ET; ++t) {
        const int pos = erow + t * 16 + mloc;
        u32x2 o;
        o[0] = pack_bf16(acc[t][0] * qs, acc[t][1] * qs);
        o[1] = pack_bf16(acc[t][2] * qs, acc[t][3] * qs);
        *(u32x2*)(out + (size_t)pos * DM + gtile * 16 + nq) = o;
      }
    } else {
#pragma unroll
      for (int t = 0; t < C::ET; ++t) {
        const int pos = erow + t * 16 + mloc;
        const int r = pos & 31;
        char* rec = kvout + (pos >> 5) * REC;
        const unsigned w0 = pack_bf16(acc[t][0], acc[t][1]), w1 = pack_bf16(acc[t][2], acc[t][3]);
        if (gtile == 36) {
          *(u32x2*)(rec + (g >> 1) * 512 + r * 16 + (g & 1) * 8) = u32x2{w0, w1};
        } else {
          const int s = r >> 4, rr = r & 15;
          const int hh = (rr >> 2) & 1, j = ((rr >> 3) << 2) | (rr & 3);
          char* vb = rec + REC_V + s * 608 + hh * 304 + j * 2;
          const int d0 = (gtile - 37) * 16 + nq - 2;
          if (d0 < 0) *(unsigned*)(rec + REC_K1 + r * 4) = w0;
          else if (d0 < 18) {
            *(u16*)(vb + d0 * 16) = (u16)(w0 & 0xffffu);
            *(u16*)(vb + (d0 + 1) * 16) = (u16)(w0 >> 16);
          } else if (d0 == 18) *(u16*)(vb + 18 * 16) = (u16)0x3f80;
          if (d0 + 2 < 18) {
            *(u16*)(vb + (d0 + 2) * 16) = (u16)(w1 & 0xffffu);
            *(u16*)(vb + (d0 + 3) * 16) = (u16)(w1 >> 16);
          }
        }
      }
    }
  } else {
#pragma unroll
    for (int t = 0; t < C::RT; ++t) {
      u32x2 o;
      o[0] = pack_bf16(acc[t][0], acc[t][1]);
      o[1] = pack_bf16(acc[t][2], acc[t][3]);
      *(u32x2*)(smem + (t * 16 + mloc) * LDQ + (tcol * 16 + nq) * 2) = o;
    }
    if (wave == 12 || wave == 13) {
      const int r = (wave - 12) * 64 + lane;
      if (r < 80) *(u32x2*)(smem + r * LDQ + 448) = u32x2{0x3f803f80u, 0x3f803f80u};
    }
    __syncthreads();
    auto units = [&](auto nuc) {
      constexpr int NU = decltype(nuc)::value;
      u32x4 qf[NU], kf0[NU], kf1[NU], vf0[NU], vf1[NU];
#pragma unroll
      for (int w = 0; w < NU; ++w) {
        const char* ub = smem + w * 32 * LDQ;
        qf[w][0] = *(const unsigned*)(ub + oq0);
#pragma unroll
        for (int j = 0; j < 4; ++j) {
          if (j) qf[w][j] = ((const unsigned*)(ub + oq1))[j];
          kf0[w][j] = ((const unsigned*)(ub + ok0))[j];
          kf1[w][j] = ((const unsigned*)(ub + ok1))[j];
        }
      }
#pragma unroll
      for (int w = 0; w < NU; ++w) {
        const char* ub = smem + w * 32 * LDQ;
#pragma unroll
        for (int h = 0; h < 2; ++h) {
          const u32x2 a = tr_read(ub + ov0 + h * 16 * LDQ), b = tr_read(ub + ov1 + h * 16 * LDQ);
          vf0[w][2 * h] = a[0];
          vf0[w][2 * h + 1] = a[1];
          vf1[w][2 * h] = b[0];
          vf1[w][2 * h + 1] = b[1];
        }
      }
#pragma unroll
      for (int w = 0; w < NU; ++w) {
        const f32x4 z4 = {0.f, 0.f, 0.f, 0.f};
        f32x4 s0 = __builtin_amdgcn_mfma_f32_16x16x32_bf16(__builtin_bit_cast(bf16x8, kf0[w]), __builtin_bit_cast(bf16x8, qf[w]), bias0, 0, 0, 0);
        f32x4 s1 = __builtin_amdgcn_mfma_f32_16x16x32_bf16(__builtin_bit_cast(bf16x8, kf1[w]), __builtin_bit_cast(bf16x8, qf[w]), bias1, 0, 0, 0);
        const float sc = 0.23570226039551584f * 1.4426950408889634f;
        s0 *= sc;
        s1 *= sc;
        f32x4 lg = {fmaxf(s0[0], s0[1]), fmaxf(s0[2], s0[3]), fmaxf(s1[0], s1[1]), fmaxf(s1[2], s1[3])};
        float mx = fmaxf(fmaxf(lg[0], lg[1]), fmaxf(lg[2], lg[3]));
        {
          const unsigned mb = __builtin_bit_cast(unsigned, mx);
          const auto r = __builtin_amdgcn_permlane16_swap(mb, mb, false, false);
          unsigned mc;
          asm("v_max_f32 %0, %1, %2" : "=v"(mc) : "v"(r[0]), "v"(r[1]));
          const auto r2 = __builtin_amdgcn_permlane32_swap(mc, mc, false, false);
          asm("v_max_f32 %0, %1, %2" : "=v"(mx) : "v"(r2[0]), "v"(r2[1]));
        }
        lg -= mx;
        u32x4 pf;
#pragma unroll
        for (int e = 0; e < 4; ++e) {
          const float pe = __builtin_amdgcn_exp2f(lg[e]);
          pf[e] = pack_bf16(pe, pe) & pm;
        }
        const f32x4 o1 = __builtin_amdgcn_mfma_f32_16x16x32_bf16(__builtin_bit_cast(bf16x8, vf1[w]), __builtin_bit_cast(bf16x8, pf), z4, 0, 0, 0);
        const f32x4 o0 = __builtin_amdgcn_mfma_f32_16x16x32_bf16(__builtin_bit_cast(bf16x8, vf0[w]), __builtin_bit_cast(bf16x8, pf), z4, 0, 0, 0);
        const unsigned db = __builtin_bit_cast(unsigned, o1[0]);
        const float inv = __builtin_amdgcn_rcpf(__builtin_bit_cast(float, (unsigned)__builtin_amdgcn_permlane16_swap(db, db, false, false)[1]));
        unsigned* op = (unsigned*)((char*)out + (unsigned)oo + w * 32 * DM * 2);
        const u32x2 wa = {pack_bf16(o0[0] * inv, o0[1] * inv), pack_bf16(o0[2] * inv, o0[3] * inv)};
        const u32x2 wb = {pack_bf16(o1[0] * inv, o1[1] * inv), pack_bf16(o1[2] * inv, o1[3] * inv)};
        if (wave & 1) {
          if (g == 0) {
            op[1] = wa[1];
            *(u32x2*)(op + 8) = wb;
          } else {
            *(u32x2*)op = wa;
          }
        } else {
          *(u32x2*)op = wa;
          if (g == 0) op[8] = wb[0];
        }
      }
    };
#pragma nounroll
    for (int it = ((wave & ~1) == 6) ? 0 : 1;; ++it) {
      units(std::integral_constant<int, 1>{});
      if (it) break;
      oq0 += 32 * LDQ; oq1 += 32 * LDQ; ok0 += 32 * LDQ; ok1 += 32 * LDQ; ov0 += 32 * LDQ; ov1 += 32 * LDQ;
      oo += 32 * DM * 2;
    }
  }
}

#define MFMA32(a, b, c) __builtin_amdgcn_mfma_f32_32x32x16_bf16(a, b, c, 0, 0, 0)
#define MFMA8(a, b, c) __builtin_amdgcn_mfma_f32_32x32x8bf16_1k(__builtin_bit_cast(bf16x4, a), __builtin_bit_cast(bf16x4, b), c, 0, 0, 0)
#define EXP2(x) __builtin_amdgcn_exp2f(x)
typedef __attribute__((ext_vector_type(4))) short bf16x4;

constexpr int CH2 = 8 * REC;
__global__ __launch_bounds__(1024) void attn_kernel(const u16* __restrict__ qin, const char* __restrict__ kvin,
                                                    u16* __restrict__ out) {
  __shared__ __attribute__((aligned(16))) char smem[66 * REC];
  __shared__ unsigned pair_cnt[8];
  const int tid = threadIdx.x, lane = tid & 63, wave = __builtin_amdgcn_readfirstlane(tid >> 6);
  const int pw = wave & 7, half = wave >> 3;
  const int r = lane & 31, h = lane >> 5;
  const int hb = half * 32 * REC;
  u32x4 st[3];
  int soff[3];
#pragma unroll
  for (int j = 0; j < 3; ++j) {
    const int o = (pw + 8 * j) * 1024 + lane * 16;
    soff[j] = hb + (o < CH2 ? o : lane * 16);
  }
#pragma unroll
  for (int j = 0; j < 3; ++j) st[j] = *(const u32x4*)(kvin + soff[j]);
  const int pos = blockIdx.x * 8 + pw;
  const u16* qp = qin + (size_t)pos * DM + r * 18;
  u32x4 q0u = *(const u32x4_a4*)(qp + h * 8);
  unsigned q1w = *(const unsigned*)(qp + 16);
  if (h) q1w = 0u;
  const bf16x8 q0 = __builtin_bit_cast(bf16x8, q0u);
  const u32x2 q1f = {q1w, 0u};
#pragma unroll
  for (int j = 0; j < 3; ++j) *(u32x4*)(smem + soff[j]) = st[j];
  if (tid < 8) pair_cnt[tid] = 0u;
  __syncthreads();
#pragma unroll
  for (int j = 0; j < 3; ++j) st[j] = *(const u32x4*)(kvin + CH2 + soff[j]);
  __builtin_amdgcn_sched_barrier(0);

  const char* k0p = smem + hb + h * 512 + r * 16;
  const char* k1p = smem + hb + (h ? REC_V + 18 * 16 : REC_K1 + r * 4);
  const char* vp = smem + hb + REC_V + h * 304 + (r < 18 ? r : 18) * 16;

  f32x16 zero;
#pragma unroll
  for (int i = 0; i < 16; ++i) zero[i] = 0.f;
  f32x16 acc = zero;
  f32x16 sA, sB;
  float m;
  u32x2 q1;
  {
    bf16x8 k0 = *(const bf16x8*)(k0p);
    u32x2 k1 = {*(const unsigned*)(k1p), 0u};
    f32x16 s = MFMA32(k0, q0, zero);
    s = MFMA8(k1, q1f, s);
    float mx = s[0];
#pragma unroll
    for (int i = 1; i < 16; ++i) mx = fmaxf(mx, s[i]);
    mx = fmaxf(mx, __shfl_xor(mx, 32));
    const float mhi = round_bf16(mx), mlo = round_bf16(mx - mhi);
    m = mhi + mlo;
    q1 = u32x2{h ? pack_bf16(-mhi, -mlo) : q1w, 0u};
#pragma unroll
    for (int i = 0; i < 16; ++i) sA[i] = s[i] - m;
  }
  bf16x8 k0A = *(const bf16x8*)(k0p + REC), k0B;
  u32x2 k1A = {*(const unsigned*)(k1p + REC), 0u}, k1B = {0u, 0u};
  bf16x8 v0A = {0, 0, 0, 0, 0, 0, 0, 0}, v1A = v0A, v0B, v1B;
  u32x4 pA0 = {0u, 0u, 0u, 0u}, pA1 = pA0, pB0, pB1;

#define SB __builtin_amdgcn_sched_barrier(0);
#define TILE_BODY(OFF, SC, SN, K0C, K1C, V0C, V1C, K0N, K1N, V0N, V1N, PP0, PP1, PC0, PC1)                     \
  {                                                                                                            \
    __builtin_amdgcn_s_waitcnt(0xc07f);                                                                        \
    SB                                                                                                         \
    acc = MFMA32(V0C, __builtin_bit_cast(bf16x8, PP0), acc);                                                   \
    K0N = *(const bf16x8*)(k0p + (OFF) + 2 * REC);                                                             \
    K1N[0] = *(const unsigned*)(k1p + (OFF) + 2 * REC);                                                        \
    V0N = *(const bf16x8*)(vp + (OFF));                                                                        \
    V1N = *(const bf16x8*)(vp + (OFF) + 608);                                                                  \
    SB                                                                                                         \
    PC0[0] = pack_bf16(EXP2(SC[0]), EXP2(SC[1]));                                                              \
    PC0[1] = pack_bf16(EXP2(SC[2]), EXP2(SC[3]));                                                              \
    SB                                                                                                         \
    acc = MFMA32(V1C, __builtin_bit_cast(bf16x8, PP1), acc);                                                   \
    SB                                                                                                         \
    PC0[2] = pack_bf16(EXP2(SC[4]), EXP2(SC[5]));                                                              \
    PC0[3] = pack_bf16(EXP2(SC[6]), EXP2(SC[7]));                                                              \
    SB                                                                                                         \
    SN = MFMA32(K0C, q0, zero);                                                                                \
    SB                                                                                                         \
    PC1[0] = pack_bf16(EXP2(SC[8]), EXP2(SC[9]));                                                              \
    PC1[1] = pack_bf16(EXP2(SC[10]), EXP2(SC[11]));                                                            \
    SB                                                                                                         \
    SN = MFMA8(K1C, q1, SN);                                                                                   \
    SB                                                                                                         \
    PC1[2] = pack_bf16(EXP2(SC[12]), EXP2(SC[13]));                                                            \
    PC1[3] = pack_bf16(EXP2(SC[14]), EXP2(SC[15]));                                                            \
    SB                                                                                                         \
  }

  for (int t = 0; t < 32; t += 4) {
    if ((t & 7) == 4 && t < 24) {
      const int c = (t >> 3) + 1;
#pragma unroll
      for (int j = 0; j < 3; ++j) *(u32x4*)(smem + c * CH2 + soff[j]) = st[j];
      if (c < 3) {
#pragma unroll
        for (int j = 0; j < 3; ++j) st[j] = *(const u32x4*)(kvin + (c + 1) * CH2 + soff[j]);
      }
      asm volatile("s_waitcnt lgkmcnt(0)" ::: "memory");
      __builtin_amdgcn_s_barrier();
      asm volatile("" ::: "memory");
    }
    const int off = t * REC;
    TILE_BODY(off, sA, sB, k0A, k1A, v0A, v1A, k0B, k1B, v0B, v1B, pA0, pA1, pB0, pB1)
    TILE_BODY(off + REC, sB, sA, k0B, k1B, v0B, v1B, k0A, k1A, v0A, v1A, pB0, pB1, pA0, pA1)
    TILE_BODY(off + 2 * REC, sA, sB, k0A, k1A, v0A, v1A, k0B, k1B, v0B, v1B, pA0, pA1, pB0, pB1)
    TILE_BODY(off + 3 * REC, sB, sA, k0B, k1B, v0B, v1B, k0A, k1A, v0A, v1A, pB0, pB1, pA0, pA1)
  }
  const float* xp = (const float*)(smem + (hb ^ (32 * REC)) + pw * 3072) + lane;
  const unsigned seen = __hip_atomic_load(&pair_cnt[pw], __ATOMIC_RELAXED, __HIP_MEMORY_SCOPE_WORKGROUP);
  asm volatile("" ::: "memory");
  float xv[12];
#pragma unroll
  for (int i = 0; i < 12; ++i) xv[i] = xp[i * 64];
  acc = MFMA32(v0A, __builtin_bit_cast(bf16x8, pA0), acc);
  acc = MFMA32(v1A, __builtin_bit_cast(bf16x8, pA1), acc);

  {
    bool bad = !(acc[10] < 1.0e30f);
#pragma unroll
    for (int i = 0; i < 10; ++i) bad = bad || !(fabsf(acc[i]) < 3.0e38f);
    if (__any(bad)) {
      acc = zero;
      m = -3.0e38f;
      const char* gk0 = kvin + (k0p - smem);
      const char* gk1 = kvin + (k1p - smem);
      const char* gv = kvin + (vp - smem);
      for (int t = 0; t < 32; ++t) {
        bf16x8 k0 = *(const bf16x8*)(gk0 + t * REC);
        u32x2 k1 = {*(const unsigned*)(gk1 + t * REC), 0u};
        bf16x8 v0 = *(const bf16x8*)(gv + t * REC);
        bf16x8 v1 = *(const bf16x8*)(gv + t * REC + 608);
        f32x16 s = MFMA32(k0, q0, zero);
        s = MFMA8(k1, q1f, s);
        float mx = s[0];
#pragma unroll
        for (int i = 1; i < 16; ++i) mx = fmaxf(mx, s[i]);
        mx = fmaxf(mx, __shfl_xor(mx, 32));
        const float mnew = fmaxf(m, mx);
        const float sc = EXP2(m - mnew);
        m = mnew;
#pragma unroll
        for (int i = 0; i < 16; ++i) acc[i] *= sc;
        u32x4 p0, p1;
#pragma unroll
        for (int i = 0; i < 4; ++i) {
          p0[i] = pack_bf16(EXP2(s[2 * i] - m), EXP2(s[2 * i + 1] - m));
          p1[i] = pack_bf16(EXP2(s[8 + 2 * i] - m), EXP2(s[8 + 2 * i + 1] - m));
        }
        acc = MFMA32(v0, __builtin_bit_cast(bf16x8, p0), acc);
        acc = MFMA32(v1, __builtin_bit_cast(bf16x8, p1), acc);
      }
    }
  }
  if (__builtin_amdgcn_readfirstlane(seen) == 0u) {
    float* wp = (float*)(smem + hb + pw * 3072) + lane;
#pragma unroll
    for (int i = 0; i < 11; ++i) wp[i * 64] = acc[i];
    wp[11 * 64] = m;
    unsigned arrived = 0u;
    if (lane == 0) arrived = __hip_atomic_fetch_add(&pair_cnt[pw], 1u, __ATOMIC_ACQ_REL, __HIP_MEMORY_SCOPE_WORKGROUP);
    arrived = __builtin_amdgcn_readfirstlane(arrived);
    if (arrived == 0u) return;
    __builtin_amdgcn_fence(__ATOMIC_ACQUIRE, "workgroup");
#pragma unroll
    for (int i = 0; i < 12; ++i) xv[i] = xp[i * 64];
  }
  {
#pragma clang fp contract(off)
    const float m1 = xv[11];
    const float mm = fmaxf(m, m1);
    const float a0 = EXP2(m - mm), a1 = EXP2(m1 - mm);
#pragma unroll
    for (int i = 0; i < 11; ++i) acc[i] = acc[i] * a0 + xv[i] * a1;
  }
  const float inv = 1.f / acc[10];
  u16* op = out + (size_t)pos * DM + r * 18;
  {
    unsigned a0 = pack_bf16(acc[0] * inv, acc[1] * inv), a1 = pack_bf16(acc[2] * inv, acc[3] * inv);
    unsigned b0 = pack_bf16(acc[4] * inv, acc[5] * inv), b1 = pack_bf16(acc[6] * inv, acc[7] * inv);
    unsigned* o32 = (unsigned*)(op + h * 4);
    o32[0] = a0;
    o32[1] = a1;
    o32[4] = b0;
    o32[5] = b1;
    if (!h) *(unsigned*)(op + 16) = pack_bf16(acc[8] * inv, acc[9] * inv);
  }
}

extern "C" void kernel_launch(void* const* d_in, const int* in_sizes, int n_in, void* d_out, int out_size, void* d_ws,
                              size_t ws_size, hipStream_t stream) {
  const u16* x0 = (const u16*)d_in[0];
  const u16* wq = (const u16*)d_in[1];
  const u16* wk = (const u16*)d_in[2];
  const u16* wv = (const u16*)d_in[3];
  const u16* woa = (const u16*)d_in[4];
  const u16* wqkv = (const u16*)d_in[6];
  const u16* wob = (const u16*)d_in[7];
  char* ws = (char*)d_ws;
  u16* x1 = (u16*)(ws + OFF_X1);
  u16* x2 = (u16*)(ws + OFF_X2);
  u16* x3 = (u16*)(ws + OFF_X3);

  prep_kernel<<<(PREP_WAVES + 3) / 4, 256, 0, stream>>>(wq, wk, wv, woa, wqkv, wob, ws);
  for (int layer = 0; layer < 2; ++layer) {
    const u16* xin = layer == 0 ? x0 : x2;
    u16* xmid = layer == 0 ? x1 : x3;
    u16* xout = layer == 0 ? x2 : (u16*)d_out;
    u16* qb = (u16*)(ws + (layer == 0 ? OFF_Q : OFF_ALT));
    char* kvb = ws + (layer == 0 ? OFF_KV : OFF_ALT + ACT_BYTES);
    u16* obuf = (u16*)(ws + (layer == 0 ? OFF_O : OFF_ALT + 2 * ACT_BYTES));
    u16* obuf2 = (u16*)(ws + OFF_ALT + (3 + layer) * ACT_BYTES);
    gemm_kernel<0><<<256, 640, 0, stream>>>(xin, ws + OFF_WA, (const u16*)(ws + OFF_CSA), qb, kvb);
    attn_kernel<<<256, 1024, 0, stream>>>(qb, kvb, obuf);
    gemm_kernel<1><<<256, 640, 0, stream>>>(obuf, ws + OFF_WOA, xin, xmid, nullptr);
    gemm_kernel<2><<<256, 896, 0, stream>>>(xmid, ws + OFF_WB, (const u16*)(ws + OFF_CSB), obuf2, nullptr);
    gemm_kernel<1><<<256, 640, 0, stream>>>(obuf2, ws + OFF_WOB, xmid, xout, nullptr);
  }
}
```
